```python
import torch

def _softmax(x, axis=-1):
    return torch.softmax(x, dim=axis)
def _gelu(x):
    return torch.nn.functional.gelu(x, approximate="tanh")
def _silu(x):
    return torch.nn.functional.silu(x)
def _sigmoid(x):
    return torch.sigmoid(x)
def _layernorm(x, axis=-1, eps=1e-5):
    m = x.mean(dim=axis, keepdim=True)
    v = x.var(dim=axis, keepdim=True, unbiased=False)
    return (x - m) / torch.sqrt(v + eps)
def _scatter_add(tgt, idx, src, axis=0):
    return tgt.clone().index_add_(axis, idx, src)
def _segment_sum(src, seg, n):
    out = torch.zeros((n,) + src.shape[1:], dtype=src.dtype)
    return out.index_add_(0, seg, src)
def _pad(x, pw):
    flat = tuple(v for pair in reversed(pw) for v in pair)
    return torch.nn.functional.pad(x, flat)

kernel_name = 'sequential_preLN__compressed_kv_cla__dense_activation_silu__prompt'

def setup_inputs():

    _w = (lambda a: (lambda t: t.to(torch.bfloat16) if t.is_floating_point() else t)(torch.as_tensor(a)))
    def _A_setup():
        B, H, Sq, Sk, D, M = 1, 16, 2048, 2048, 256, 448
        Dc = 4 * D
        return {
            "x": _w((lambda *s: torch.randn(*s, dtype=torch.bfloat16) * 0.05)(B, Sq, M)),
            "c_kv": _w((lambda *s: torch.randn(*s, dtype=torch.bfloat16) * 0.4)(B, Sk, Dc)),
            "w_q": _w((lambda *s: torch.randn(*s, dtype=torch.bfloat16) * 1.6)(M, H, D) * (M ** -0.5)),
            "w_uk": _w((lambda *s: torch.randn(*s, dtype=torch.bfloat16) * 1.6)(Dc, H, D) * (Dc ** -0.5)),
            "w_uv": _w((lambda *s: torch.randn(*s, dtype=torch.bfloat16) * 0.4)(Dc, H, D) * (Dc ** -0.5)),
            "w_o": _w((lambda *s: torch.randn(*s, dtype=torch.bfloat16) * 0.4)(H, D, M) * ((H * D) ** -0.5)),
        }
    _A_in = _A_setup()

    def _B_setup():
        B, S, D, F = 1, 2048, 448, 1792
        return {
            "x": _w((lambda *s: torch.randn(*s, dtype=torch.bfloat16) * 0.05)(B, S, D)),
            "w1": _w((lambda *s: torch.randn(*s, dtype=torch.bfloat16) * 0.8)(D, F) * (D ** -0.5)),
            "w2": _w((lambda *s: torch.randn(*s, dtype=torch.bfloat16) * 0.1)(F, D) * (F ** -0.5)),
        }
    _B_in = _B_setup()

    return {
        "A_x": _A_in["x"],
        "A_c_kv": _A_in["c_kv"],
        "A_w_q": _A_in["w_q"],
        "A_w_uk": _A_in["w_uk"],
        "A_w_uv": _A_in["w_uv"],
        "A_w_o": _A_in["w_o"],
        "B_x": _B_in["x"],
        "B_w1": _B_in["w1"],
        "B_w2": _B_in["w2"],
    }

def reference(A_x, A_c_kv, A_w_q, A_w_uk, A_w_uv, A_w_o, B_x, B_w1, B_w2):
    def _A_ref(x, c_kv, w_q, w_uk, w_uv, w_o):
        B, Sq, M = x.shape[0], x.shape[1], x.shape[2]
        Sk = c_kv.shape[1]
        H, D = w_q.shape[1], w_q.shape[2]
        q = torch.einsum("bqm,mhd->bhqd", x, w_q)
        k = torch.einsum("bkc,chd->bhkd", c_kv, w_uk)
        v = torch.einsum("bkc,chd->bhkd", c_kv, w_uv)
        scores = torch.einsum("bhqd,bhkd->bhqk", q, k) * (D ** -0.5)
        off = Sk - Sq
        qi = torch.arange(Sq)[:, None]
        kj = torch.arange(Sk)[None, :]
        cmask = kj <= (qi + off)
        scores = torch.where(cmask[None, None], scores, -1e9)
        p = _softmax(scores, axis=-1)
        ctx = torch.einsum("bhqk,bhkd->bhqd", p, v)
        return torch.einsum("bhqd,hdm->bqm", ctx, w_o)

    def _B_ref(x, w1, w2):
        h = torch.einsum("bsd,df->bsf", x, w1)
        h = _silu(h)
        out = torch.einsum("bsf,fd->bsd", h, w2)
        return out

    x = A_x
    h = x + _A_ref(_layernorm(x, axis=-1), A_c_kv, A_w_q, A_w_uk, A_w_uv, A_w_o)
    h2 = h + _B_ref(_layernorm(h, axis=-1), B_w1, B_w2)
    return h2
```

```cpp
#include <hip/hip_runtime.h>
#include <hip/hip_bf16.h>
#include <utility>
#pragma clang diagnostic ignored "-Winline-asm"

typedef unsigned short u16;
typedef short bf16x8 __attribute__((ext_vector_type(8)));
typedef short s16x4 __attribute__((ext_vector_type(4)));
typedef float f32x16 __attribute__((ext_vector_type(16)));
typedef float f32x4 __attribute__((ext_vector_type(4)));
typedef float f32x2 __attribute__((ext_vector_type(2)));
typedef __bf16 bf16x2_t __attribute__((ext_vector_type(2)));
typedef unsigned u32x4 __attribute__((ext_vector_type(4)));
typedef __amdgpu_buffer_rsrc_t srd_t;
#define LAS __attribute__((address_space(3)))
#define DI __device__ __forceinline__
#define INL __attribute__((always_inline))
#define GAP() __builtin_amdgcn_sched_barrier(0)
#define WAITCNT_IMM(vm, lgkm) (((vm) & 15) | (7 << 4) | (((lgkm) & 15) << 8) | ((((vm) >> 4) & 3) << 14))
#define WAIT_VL(vm, lgkm) __builtin_amdgcn_s_waitcnt(WAITCNT_IMM(vm, lgkm))
#define WAIT_L0() __builtin_amdgcn_s_waitcnt(WAITCNT_IMM(63, 0))
#define BAR() __builtin_amdgcn_s_barrier()
#define ST_AUX 17
#define LD_AUX 16
#define LD_SC " sc1"
#define SPIN_MAX (1u << 18)

constexpr int SEQ = 2048, DM = 448, DC = 1024, NH = 16, HD = 256, FF = 1792, HDT = NH * HD;

constexpr size_t WS_FLAG = 0;
constexpr int FL_HB = 0, FL_X = 8, FL_KV = 24, FL_KV0 = 40, FL_KV1 = 56, FL_Q = 72, FL_P = FL_Q + 1024, FL_C = FL_P + 512, FL_Y = FL_C + 1024, FL_H = FL_Y + 16, FL_F = FL_H + 64, FL_N = FL_F + 32;
constexpr size_t WS_XN = 1 << 20;
constexpr size_t WS_Q = WS_XN + (size_t)SEQ * DM * 2;
constexpr size_t WS_K = WS_Q + (size_t)SEQ * HDT * 2;
constexpr size_t WS_V = WS_K + (size_t)SEQ * HDT * 2;
constexpr size_t WS_CTX = WS_V + (size_t)SEQ * HDT * 2;
constexpr size_t PART_BYTES = 17 * 1024;
constexpr size_t WS_PART = WS_CTX + (size_t)SEQ * HDT * 2;
constexpr size_t WS_Y = WS_PART + 512 * PART_BYTES;
constexpr size_t WS_H = WS_Y + (size_t)8 * SEQ * DM * 2;
constexpr size_t WS_HN = WS_H + (size_t)SEQ * DM * 2;
constexpr size_t WS_H1 = WS_HN + (size_t)SEQ * DM * 2;
constexpr size_t WS_END = WS_H1 + (size_t)SEQ * FF * 2;

#define ACLOB "a0","a1","a2","a3","a4","a5","a6","a7","a8","a9","a10","a11","a12","a13","a14","a15","a16","a17","a18","a19","a20","a21","a22","a23","a24","a25","a26","a27","a28","a29","a30","a31","a32","a33","a34","a35","a36","a37","a38","a39","a40","a41","a42","a43","a44","a45","a46","a47","a48","a49","a50","a51","a52","a53","a54","a55","a56","a57","a58","a59","a60","a61","a62","a63","a64","a65","a66","a67","a68","a69","a70","a71","a72","a73","a74","a75","a76","a77","a78","a79","a80","a81","a82","a83","a84","a85","a86","a87","a88","a89","a90","a91","a92","a93","a94","a95","a96","a97","a98","a99","a100","a101","a102","a103","a104","a105","a106","a107","a108","a109","a110","a111","a112","a113","a114","a115","a116","a117","a118","a119","a120","a121","a122","a123","a124","a125","a126","a127","a128","a129","a130","a131","a132","a133","a134","a135","a136","a137","a138","a139","a140","a141","a142","a143","a144","a145","a146","a147","a148","a149","a150","a151","a152","a153","a154","a155","a156","a157","a158","a159","a160","a161","a162","a163","a164","a165","a166","a167","a168","a169","a170","a171","a172","a173","a174","a175","a176","a177","a178","a179","a180","a181","a182","a183","a184","a185","a186","a187","a188","a189","a190","a191","a192","a193","a194","a195","a196","a197","a198","a199","a200","a201","a202","a203","a204","a205","a206","a207","a208","a209","a210","a211","a212","a213","a214","a215","a216","a217","a218","a219","a220","a221","a222","a223","a224","a225","a226","a227","a228","a229","a230","a231","a232","a233","a234","a235","a236","a237","a238","a239","a240","a241","a242","a243","a244","a245","a246","a247","a248","a249","a250","a251","a252","a253","a254","a255"
DI void AMFMA(int idx, bf16x8 a, bf16x8 b) { asm volatile("v_mfma_f32_16x16x32_bf16 a[%c0:%c1], %2, %3, a[%c0:%c1]" :: "i"(idx), "i"(idx + 3), "v"(a), "v"(b)); }
DI void AMFMA32(int idx, bf16x8 a, bf16x8 b) { asm volatile("v_mfma_f32_32x32x16_bf16 a[%c0:%c1], %2, %3, a[%c0:%c1]" :: "i"(idx), "i"(idx + 15), "v"(a), "v"(b)); }
DI void AMFMA32Z(int idx, bf16x8 a, bf16x8 b) { asm volatile("v_mfma_f32_32x32x16_bf16 a[%c0:%c1], %2, %3, 0" :: "i"(idx), "i"(idx + 15), "v"(a), "v"(b)); }
DI void SMFMA0(f32x4& d, bf16x8 a, bf16x8 b, f32x4 cin) { asm volatile("v_mfma_f32_16x16x32_bf16 %0, %1, %2, %3" : "=&v"(d) : "v"(a), "v"(b), "v"(cin)); }
DI void SMFMAZ(f32x4& d, bf16x8 a, bf16x8 b) { asm volatile("v_mfma_f32_16x16x32_bf16 %0, %1, %2, 0" : "=&v"(d) : "v"(a), "v"(b)); }
DI void SMFMA(f32x4& d, bf16x8 a, bf16x8 b) { asm volatile("v_mfma_f32_16x16x32_bf16 %0, %1, %2, %0" : "+v"(d) : "v"(a), "v"(b)); }
DI void VMFMA32_A(f32x16& d, bf16x8 a, int bidx) { asm volatile("v_mfma_f32_32x32x16_bf16 %0, %1, a[%c2:%c3], %0" : "+v"(d) : "v"(a), "i"(bidx), "i"(bidx + 3)); }
DI void VMFMA32_AZ(f32x16& d, bf16x8 a, int bidx) { asm volatile("v_mfma_f32_32x32x16_bf16 %0, %1, a[%c2:%c3], 0" : "=&v"(d) : "v"(a), "i"(bidx), "i"(bidx + 3)); }
DI void SMFMA_A(f32x4& d, bf16x8 a, int bidx) { asm volatile("v_mfma_f32_16x16x32_bf16 %0, %1, a[%c2:%c3], %0" : "+v"(d) : "v"(a), "i"(bidx), "i"(bidx + 3)); }
DI void SMFMA_AZ(f32x4& d, bf16x8 a, int bidx) { asm volatile("v_mfma_f32_16x16x32_bf16 %0, %1, a[%c2:%c3], 0" : "=&v"(d) : "v"(a), "i"(bidx), "i"(bidx + 3)); }
DI void AZERO4(int idx) {
  asm volatile("v_accvgpr_write_b32 a[%c0], 0\n\tv_accvgpr_write_b32 a[%c1], 0\n\tv_accvgpr_write_b32 a[%c2], 0\n\tv_accvgpr_write_b32 a[%c3], 0" :: "i"(idx), "i"(idx + 1), "i"(idx + 2), "i"(idx + 3));
}
DI void AWRITE1(int idx, float src) { asm volatile("v_accvgpr_write_b32 a[%c0], %1" :: "i"(idx), "v"(src)); }
template <class T> DI void AREAD1(T& dst, int idx) { asm volatile("v_accvgpr_read_b32 %0, a[%c1]" : "=v"(dst) : "i"(idx)); }
DI f32x4 aread(int idx) {
  float t0, t1, t2, t3;
  asm volatile("v_accvgpr_read_b32 %0, a[%c4]\n\tv_accvgpr_read_b32 %1, a[%c5]\n\tv_accvgpr_read_b32 %2, a[%c6]\n\tv_accvgpr_read_b32 %3, a[%c7]"
               : "=v"(t0), "=v"(t1), "=v"(t2), "=v"(t3) : "i"(idx), "i"(idx + 1), "i"(idx + 2), "i"(idx + 3));
  return f32x4{t0, t1, t2, t3};
}
template <int... Is, class F> DI void static_for_(std::integer_sequence<int, Is...>, F&& f) { (f(std::integral_constant<int, Is>{}), ...); }
template <int N, class F> DI void static_for(F&& f) { static_for_(std::make_integer_sequence<int, N>{}, f); }
template <class T> DI void PIN(T& x) { asm volatile("" : "+v"(x)); }
template <class T> DI void PINS(T& x) { asm volatile("" : "+s"(x)); }
DI void vset(unsigned& d, unsigned v) { asm volatile("v_mov_b32 %0, %1" : "+v"(d) : "v"(v)); }
DI unsigned rfl(unsigned x) { return __builtin_amdgcn_readfirstlane(x); }
DI unsigned cvtpk(float lo, float hi) { f32x2 v = {lo, hi}; return __builtin_bit_cast(unsigned, __builtin_convertvector(v, bf16x2_t)); }
DI srd_t mk_srd(const void* p, unsigned bytes) { return __builtin_amdgcn_make_buffer_rsrc((void*)p, (short)0, (int)bytes, 0x00020000); }
DI void set_m0(unsigned v) { asm volatile("s_mov_b32 m0, %0" ::"s"(v) : "memory", ACLOB); }
template <int OFF> DI void dma16(unsigned voff, srd_t srd, unsigned soff) {
  asm volatile("buffer_load_dwordx4 %0, %1, %2 offen offset:%3 lds" ::"v"(voff), "s"(srd), "s"(soff), "i"(OFF) : "memory");
}
template <int OFF> DI void dma16s(unsigned voff, srd_t srd, unsigned soff) {
  asm volatile("buffer_load_dwordx4 %0, %1, %2 offen offset:%3" LD_SC " lds" ::"v"(voff), "s"(srd), "s"(soff), "i"(OFF) : "memory");
}
DI void dma_grp(const unsigned (&d)[4], srd_t s, unsigned so, unsigned m) {
  set_m0(m); asm volatile("s_nop 0");
  dma16<0>(d[0], s, so); dma16<1024>(d[1], s, so); dma16<2048>(d[2], s, so); dma16<3072>(d[3], s, so);
}
DI void aload16s(int idx, unsigned voff, srd_t srd, unsigned soff, int off) {
  asm volatile("buffer_load_dwordx4 a[%c0:%c1], %2, %3, %4 offen offset:%5" LD_SC ::"i"(idx), "i"(idx + 3), "v"(voff), "s"(srd), "s"(soff), "i"(off) : "memory");
}
DI void vload16(bf16x8& dst, unsigned voff, srd_t srd, unsigned soff, int off) {
  asm volatile("buffer_load_dwordx4 %0, %1, %2, %3 offen offset:%4" : "=v"(dst) : "v"(voff), "s"(srd), "s"(soff), "i"(off) : "memory");
}
DI void vload16s(bf16x8& dst, unsigned voff, srd_t srd, unsigned soff, int off) {
  asm volatile("buffer_load_dwordx4 %0, %1, %2, %3 offen offset:%4" LD_SC : "=v"(dst) : "v"(voff), "s"(srd), "s"(soff), "i"(off) : "memory");
}
DI void asm_ld(bf16x8& dst, unsigned voff, srd_t srd, unsigned soff) {
  asm volatile("buffer_load_dwordx4 %0, %1, %2, %3 offen" LD_SC : "=v"(dst) : "v"(voff), "s"(srd), "s"(soff) : "memory");
}
DI void touch(unsigned voff, srd_t srd) {
  asm volatile("buffer_load_dword v254, %0, %1, 0 offen" :: "v"(voff), "s"(srd) : "memory", "v254");
}
DI void st16(u32x4 v, srd_t srd, unsigned voff, unsigned soff) {
  __builtin_amdgcn_raw_buffer_store_b128(v, srd, voff, soff, ST_AUX);
  asm volatile("s_nop 1" ::"v"(v) : "memory");
}
DI void st16g(u32x4 v, srd_t srd, unsigned voff, unsigned soff) {
  __builtin_amdgcn_raw_buffer_store_b128(v, srd, voff, soff, ST_AUX); GAP();
}
DI u32x4 ld16(srd_t srd, unsigned voff, unsigned soff) { return __builtin_amdgcn_raw_buffer_load_b128(srd, voff, soff, LD_AUX); }
DI bf16x8 lds128(unsigned addr) { return *(const LAS bf16x8*)(size_t)addr; }
DI s16x4 ldstr(unsigned addr) {
  return __builtin_bit_cast(s16x4, __builtin_amdgcn_ds_read_tr16_b64_v4i16((LAS s16x4*)(size_t)addr));
}
DI bf16x8 cat(s16x4 lo, s16x4 hi) { return __builtin_shufflevector(lo, hi, 0, 1, 2, 3, 4, 5, 6, 7); }
DI u32x4 pack2(const f32x4& a, const f32x4& b) { return u32x4{cvtpk(a[0], a[1]), cvtpk(a[2], a[3]), cvtpk(b[0], b[1]), cvtpk(b[2], b[3])}; }
DI float bflo(unsigned u) { return __uint_as_float(u << 16); }
DI float bfhi(unsigned u) { return __uint_as_float(u & 0xffff0000u); }
DI float dot2(unsigned a, unsigned b, float c) {
  return __builtin_amdgcn_fdot2_f32_bf16(__builtin_bit_cast(bf16x2_t, a), __builtin_bit_cast(bf16x2_t, b), c, false);
}
DI float xsum32(float x) {
  x += __uint_as_float(__builtin_amdgcn_update_dpp(0, __float_as_uint(x), 0xb1, 0xf, 0xf, false));
  x += __uint_as_float(__builtin_amdgcn_update_dpp(0, __float_as_uint(x), 0x4e, 0xf, 0xf, false));
  x += __uint_as_float(__builtin_amdgcn_update_dpp(0, __float_as_uint(x), 0x124, 0xf, 0xf, false));
  x += __uint_as_float(__builtin_amdgcn_update_dpp(0, __float_as_uint(x), 0x128, 0xf, 0xf, false));
  auto p = __builtin_amdgcn_permlane16_swap(__float_as_uint(x), __float_as_uint(x), false, false);
  return __uint_as_float(p[0]) + __uint_as_float(p[1]);
}
DI float xsum8_16_32(float x) {
  x += __uint_as_float(__builtin_amdgcn_update_dpp(0, __float_as_uint(x), 0x128, 0xf, 0xf, false));
  auto p = __builtin_amdgcn_permlane16_swap(__float_as_uint(x), __float_as_uint(x), false, false);
  x = __uint_as_float(p[0]) + __uint_as_float(p[1]);
  auto q = __builtin_amdgcn_permlane32_swap(__float_as_uint(x), __float_as_uint(x), false, false);
  return __uint_as_float(q[0]) + __uint_as_float(q[1]);
}
DI unsigned* flagp(char* ws, int i) { return (unsigned*)(ws + WS_FLAG) + (size_t)i * 32; }
DI void signal(unsigned* p) {
  asm volatile("s_nop 4\n\ts_mov_b64 exec, 1\n\tglobal_atomic_add %0, %1, %2\n\ts_mov_b64 exec, -1" :: "v"(0u), "v"(1u), "s"(p) : "memory");
}
DI void signal_wg(unsigned* p) {
  BAR();
  asm volatile("s_nop 4\n\tv_cmp_eq_u32 vcc, 0, %3\n\ts_nop 1\n\ts_mov_b64 exec, vcc\n\ts_cbranch_execz .Ls%=\n\tglobal_atomic_add %0, %1, %2\n.Ls%=:\n\ts_mov_b64 exec, -1"
               :: "v"(0u), "v"(1u), "s"(p), "v"(threadIdx.x) : "memory", "vcc");
}
struct Sync { char* ws; };
enum { S_X, S_KV0, S_KV1, S_KV, S_Q1, S_Q2, S_P, S_C, S_Y, S_H, S_F0, S_F1, S_N };
DI void heartbeat(char* ws) { signal((unsigned*)ws + blockIdx.x); }
DI unsigned noted(const Sync&, int k) { unsigned r; asm volatile("v_readlane_b32 %0, v251, %1\n\ts_nop 1" : "=s"(r) : "i"(k)); return r; }
template <int RN, int RO> DI unsigned poll_step(unsigned* p) {
  unsigned r;
  asm volatile("s_sleep 3\n\tglobal_load_dword v[%c3], %1, %2" LD_SC "\n\ts_waitcnt vmcnt(1)\n\tv_readfirstlane_b32 %0, v[%c4]\n\ts_nop 1"
               : "=s"(r) : "v"(0u), "s"(p), "i"(RN), "i"(RO) : "memory", "v249", "v250");
  return r;
}
DI void await(const Sync& y, int k, unsigned* p, unsigned want) {
  const unsigned base = noted(y, k);
  asm volatile("s_nop 4\n\tglobal_load_dword v249, %0, %1" LD_SC :: "v"(0u), "s"(p) : "memory", "v249");
#pragma unroll 1
  for (unsigned spins = 0; spins < SPIN_MAX; ++spins) {
    if (poll_step<250, 249>(p) - base >= want) break;
    if (poll_step<249, 250>(p) - base >= want) break;
    if ((spins & 31) == 31) { heartbeat(y.ws); asm volatile("s_waitcnt vmcnt(0)" ::: "memory"); }
  }
  __builtin_amdgcn_fence(__ATOMIC_ACQUIRE, "wavefront");
}
DI void await_wg(const Sync& y, int k, unsigned* p, unsigned want, unsigned w) { if (w == 0) await(y, k, p, want); BAR(); GAP(); }
template <int R> DI void peek_issue(unsigned* p) { asm volatile("s_nop 4\n\tglobal_load_dword v[%c2], %0, %1" LD_SC :: "v"(0u), "s"(p), "i"(R) : "memory", "v254", "v255"); }
template <int R> DI unsigned peek_value(const Sync& y, int k) { unsigned r; asm volatile("v_readfirstlane_b32 %0, v[%c1]\n\ts_nop 1" : "=s"(r) : "i"(R) : "memory"); return r - noted(y, k); }
template <int R> DI void note_issue(char* ws, unsigned voff) { asm volatile("global_load_dword v[%c2], %0, %1" LD_SC :: "v"(voff), "s"(ws), "i"(R) : "memory", "v251", "v252", "v253"); }
DI void chores(int t, unsigned mask, char* ws, unsigned vhb, unsigned vsx, unsigned vt4, unsigned vpx, unsigned one) {
  asm volatile("s_bitcmp1_b32 %1, %0\n\ts_cbranch_scc0 .Lce%=\n\t"
               "s_cmp_eq_u32 %0, 5\n\ts_cbranch_scc1 .Lc5%=\n\ts_cmp_eq_u32 %0, 13\n\ts_cbranch_scc1 .Lc13%=\n\t"
               "s_mov_b64 exec, 1\n\ts_cmp_lg_u32 %0, 8\n\ts_cbranch_scc1 .Lc1%=\n\t"
               "global_atomic_add %4, %7, %2\n\ts_mov_b64 exec, -1\n\ts_branch .Lce%=\n"
               ".Lc1%=:\n\tglobal_atomic_add %3, %7, %2\n\ts_mov_b64 exec, -1\n\ts_branch .Lce%=\n"
               ".Lc5%=:\n\tglobal_load_dword v253, %5, %2" LD_SC "\n\ts_branch .Lce%=\n"
               ".Lc13%=:\n\tglobal_load_dword v255, %6, %2" LD_SC "\n.Lce%=:"
               :: "s"(t), "s"(mask), "s"(ws), "v"(vhb), "v"(vsx), "v"(vt4), "v"(vpx), "v"(one) : "memory", "scc", "v253", "v255");
}
DI unsigned long long hb_unmoved() { unsigned long long m; asm volatile("v_cmp_eq_u32 %0, v252, v253" : "=s"(m) :: "memory"); return m; }
DI unsigned long long is7(int t) { unsigned long long m; asm volatile("s_cmp_eq_u32 %1, 7\n\ts_cselect_b64 %0, -1, 0" : "=s"(m) : "s"(t) : "scc"); return m; }
DI bool hb_all_moved() { unsigned long long m; asm volatile("v_cmp_eq_u32 %0, v252, v253\n\ts_nop 1" : "=s"(m) :: "memory"); return m == 0ull; }
DI void swap16(u32x4& a, u32x4& b) {
#pragma unroll
  for (int d = 0; d < 4; ++d) { auto p = __builtin_amdgcn_permlane16_swap(a[d], b[d], false, false); a[d] = p[0]; b[d] = p[1]; }
}

constexpr unsigned L1_A = 65536u, LDS_BYTES = 163840u;
constexpr unsigned AH = 256 * 2048;
struct Frags { bf16x8 a[8]; s16x4 blo[8], bhi[8]; };

struct KS { unsigned ba[4], aa, vB[4], vA[4], soB, soA, mB, mA; srd_t sB, sA; };
template <bool ISV, bool RD, class X>
DI void kstep1(const Frags& cur, Frags& nxt, KS& k, X&& extra) {
  static_for<64>([&](auto G_) INL {
    constexpr int g = G_, mb = g >> 3, nb = (mb & 1) ? 7 - (g & 7) : (g & 7);
    const bf16x8 b = cat(cur.blo[nb], cur.bhi[nb]);
    if constexpr (ISV) AMFMA(((mb >> 1) * 8 + nb) * 8 + (mb & 1) * 4, cur.a[mb], b);
    else               AMFMA((mb * 8 + nb) * 4, b, cur.a[mb]);
    GAP();
    if constexpr (RD && (g & 1) == 0 && g < 48) {
      constexpr int j = g >> 1;
      if constexpr (j < 16) {
        constexpr int n2 = j >> 1;
        const unsigned ad = k.ba[n2 & 3] + (n2 >> 2) * 4096 + (j & 1) * 512;
        if constexpr (j & 1) nxt.bhi[n2] = ldstr(ad); else nxt.blo[n2] = ldstr(ad);
      } else {
        nxt.a[j - 16] = lds128(k.aa + (j - 16) * 2048);
      }
    }
    if constexpr (g == 1) set_m0(k.mB);
    if constexpr (g == 33) set_m0(k.mA);
    if constexpr ((g & 7) == 3) { if constexpr (g < 32) dma16<(g >> 3) * 1024>(k.vB[g >> 3], k.sB, k.soB); else dma16<((g >> 3) & 3) * 1024>(k.vA[(g >> 3) & 3], k.sA, k.soA); }
    extra(G_);
    GAP();
  });
}

constexpr unsigned QWRAP = 14 * 262144 - 128;
struct K1 {
  unsigned rA[2], rB[4];
  unsigned dA[4], dB[4], dQ[4];
  srd_t sA, sB, sQ;
  unsigned soA, soB, soQ, limQ, mAw, ldsb, w;
};

template <bool ISV, class F, class L>
DI void kv_loop(Frags& F0, Frags& F1, const K1& c, F&& hook, L&& last) {
  unsigned aslot = 0, anext = 32768, afree = 65536;
  unsigned bn = 16384;
  unsigned stA0 = 128 - AH, stA1 = AH, stB = 262144, limA = ~0u, limB = ~0u, mAw = c.mAw, mAh = 4096;
  const unsigned mBw = c.ldsb + c.w * 4096;
  KS k;
#pragma unroll
  for (int i = 0; i < 4; ++i) { k.ba[i] = c.rB[i] + 16384; k.vB[i] = c.dB[i]; k.vA[i] = c.dA[i]; }
  k.aa = c.rA[1]; k.soB = c.soB + 4 * 262144; k.soA = c.soA + 2 * 128 + AH; k.mB = mBw; k.mA = mAw + 65536 + 4096; k.sB = c.sB; k.sA = c.sA;
  auto advance = [&](auto G_, auto SECOND_, int T) INL {
    constexpr int g = G_; constexpr bool second = SECOND_;
    if constexpr (g == 5) { k.mB = mBw + bn; PINS(k.mB); bn = (bn + 16384) & 65535; PINS(bn); }
    if constexpr (g == 29) { k.soB += stB; k.soB = k.soB >= limB ? k.soB - QWRAP : k.soB; PINS(k.soB); }
    if constexpr (g == 31 && second) {
      if (T == 13) {
        k.sB = c.sQ; k.soB = c.soQ; stB = 4 * 262144; limB = c.limQ;
#pragma unroll
        for (int i = 0; i < 4; ++i) vset(k.vB[i], c.dQ[i]);
      }
    }
    if constexpr (g == 37 || g == 39 || g == 41 || g == 45) { constexpr int i = g == 45 ? 3 : (g - 37) / 2; k.ba[i] = c.rB[i] + bn; PIN(k.ba[i]); }
    if constexpr (g == 47 && second) { const unsigned t_ = aslot; aslot = anext; anext = afree; afree = t_; }
    if constexpr (g == 48) { k.aa = second ? c.rA[1] + aslot : c.rA[0] + anext; PIN(k.aa); }
    if constexpr (g == 60) { k.soA += second ? stA1 : stA0; PINS(k.soA); }
    if constexpr (g == 62) { const bool wrap = k.soA >= limA; k.soA = wrap ? k.soA - QWRAP : k.soA; limA = wrap ? limA + 128 : limA; PINS(k.soA); PINS(limA); }
    if constexpr (g == 7) {
      if constexpr (second) {
        if (T == 13) {
          k.sA = c.sQ; k.soA = c.soQ + 2 * 262144 + 128; limA = c.limQ + 128; stA0 = 4 * 262144; stA1 = 4 * 262144; mAw = c.ldsb + L1_A + c.w * 4096; mAh = 16384;
#pragma unroll
          for (int i = 0; i < 4; ++i) vset(k.vA[i], c.dQ[i]);
        }
      }
    }
    if constexpr (g == 9) { k.mA = second ? mAw + aslot : mAw + afree + mAh; PINS(k.mA); }
  };
#pragma unroll 1
  for (int T = 0;; ++T) {
    WAIT_VL(21, 0); BAR(); GAP();
    kstep1<ISV, true>(F0, F1, k, [&](auto G_) INL { advance(G_, std::false_type{}, T); hook(G_, T); });
    WAIT_VL(18, 0); BAR(); GAP();
    if (T == 15) break;
    kstep1<ISV, true>(F1, F0, k, [&](auto G_) INL { advance(G_, std::true_type{}, T); });
  }
#pragma unroll
  for (int i = 0; i < 4; ++i) { k.vB[i] = c.dQ[i]; k.vA[i] = c.dQ[i]; }
  k.sB = c.sQ; k.sA = c.sQ;
  kstep1<ISV, false>(F1, F0, k, [&](auto G_) INL { if constexpr (G_ == 9) { k.mA = c.ldsb + L1_A + c.w * 4096; } last(G_); });
}

constexpr int QW = 7, QSIG0 = 64, QPEEK0 = 140, QPEEK = 214;
constexpr int kv_lo(int f) { return f <= 56 ? (5 * f + 1) / 2 : f >= 220 ? 364 : 140 + ((f - 56) * 224 + 163) / 164; }
constexpr int qe_lo(int f) { const int r = f % 56; return r < 3 ? 0 : r > 52 ? 58 + 0 * r : ((r - 3) * 58 + 49) / 50; }
constexpr int q_dma(int f) { return f >= 64 && f < 96 && (f & 1); }
constexpr int q_st(int f) {
  int n = 0;
  for (int m = kv_lo(f); m < kv_lo(f + 1); ++m) n += m < 364 && m % 13 == 12;
  if (f >= 56 && (f + 1) % 56) for (int m = qe_lo(f); m < qe_lo(f + 1); ++m) n += m % 29 >= 27;
  return n;
}
constexpr int q_ops(int f0, int f1) { int n = 0; for (int f = f0; f < f1; ++f) n += q_dma(f) + q_st(f); return n; }
constexpr int x_younger(int k) { return 27 - k + 8 + (k < 16 ? 8 - (k + 1) / 2 : 0); }

template <class F> DI void phase1(Sync& y, const unsigned (&sidx)[S_N], const u16* x, const u16* ckv, const u16* wq, const u16* wkv, const u16* wo, const u16* w1, const u16* w2, char* ws, unsigned ldsb, unsigned w, bool isv, int kvhead, int mt, int head, int qblk,
               srd_t sQs, u32x4 (&Qo)[8][2], F&& try_k) {
  const int lane = threadIdx.x & 63, r = lane & 15, g = lane >> 4;
  const unsigned wr = w >> 1, wc = w & 1;
  K1 c;
  c.sA = mk_srd(ckv, SEQ * DC * 2); c.sB = mk_srd(wkv, DC * HDT * 2); c.sQ = mk_srd(wq, DM * HDT * 2);
  c.ldsb = ldsb; c.w = w;
  {
    const unsigned q = (lane >> 2) & 3, p = lane & 3;
#pragma unroll
    for (int ks = 0; ks < 2; ++ks) c.rA[ks] = ldsb + L1_A + (wr * 128 + r) * 128 + (((4 * ks + g) ^ ((r >> 1) & 7)) * 16);
#pragma unroll
    for (int cc = 0; cc < 4; ++cc)
      c.rB[cc] = ldsb + wc * 8192 + g * 1024 + q * 128 + (((2 * cc + (p >> 1)) ^ (4 * (g & 1) + 2 * (q >> 1))) * 16) + (p & 1) * 8;
#pragma unroll
    for (int j = 0; j < 4; ++j) {
      c.dA[j] = (8 * j + (lane >> 3)) * 2048 + (((lane & 7) ^ (4 * (j & 1) + (lane >> 4))) * 16) - j * 1024;
      c.dB[j] = (8 * j + (lane >> 3)) * 8192 + (((lane & 7) ^ (4 * (j & 1) + 2 * ((lane >> 4) & 1))) * 16) - j * 1024;
      c.dQ[j] = (8 * j + (lane >> 3)) * 8192 + (((lane & 7) ^ (((lane >> 4) & 1) << 2)) * 16) - j * 1024;
    }
  }
  c.soA = (w * 512 + mt * 32) * 2048;
  c.soB = kvhead * 512 + w * 128;
  c.soQ = head * 512 + w * 262144; c.limQ = head * 512 + 14 * 262144;
  auto q_so = [&](int G) -> unsigned { const unsigned u = 4 * G + w, cq = u >= 42 ? 3 : u >= 28 ? 2 : u >= 14 ? 1 : 0; return head * 512 + (u - 14 * cq) * 262144 + cq * 128; };
  c.mAw = ldsb + L1_A + w * 8192;
  const unsigned mBw = ldsb + w * 4096;
  const unsigned l16 = lane * 16;
  bf16x8 xa, xb;
  const int xrow = blockIdx.x * 8 + (threadIdx.x >> 5), xc = threadIdx.x & 31;
  {
    const srd_t sx = mk_srd(x, SEQ * DM * 2);
    vload16(xa, xrow * (DM * 2) + xc * 16, sx, 0, 0);
    vload16(xb, xc < 24 ? xrow * (DM * 2) + xc * 16 : 0x7fffff00u, sx, 0, 512);
  }
  dma_grp(c.dA, c.sA, c.soA, c.mAw);
  dma_grp(c.dB, c.sB, c.soB, mBw);
  dma_grp(c.dA, c.sA, c.soA + AH, c.mAw + 4096);
  GAP();
  {
    unsigned so = 0;
#pragma unroll
    for (int k = 0; k < S_N; ++k) so = lane == k ? sidx[k] * 128 : so;
    note_issue<251>(ws, so); note_issue<252>(ws, threadIdx.x * 4);
  }
  GAP();
  dma_grp(c.dB, c.sB, c.soB + 262144, mBw + 16384);
  GAP();
  srd_t sT;
  {
    const unsigned wid = blockIdx.x * 4 + w;
    unsigned long long p0 = (unsigned long long)wq, p1 = (unsigned long long)wo, p2 = (unsigned long long)w1, p3 = (unsigned long long)w2, pc;
    unsigned len;
    PINS(p0); PINS(p1); PINS(p2); PINS(p3);
    asm volatile("s_getpc_b64 %0\n.Lpc%=:\n\ts_mov_b32 %1, k1_code_end-.Lpc%=" : "=s"(pc), "=s"(len));
    const unsigned long long base = wid < 299 ? p0 : wid < 598 ? p1 : wid < 729 ? p2 : wid < 860 ? p3 : pc;
    const unsigned size = wid < 598 ? HDT * DM * 2 : wid < 860 ? DM * FF * 2 : len;
    const unsigned off = (wid < 299 ? wid : wid < 598 ? wid - 299 : wid < 729 ? wid - 598 : wid < 860 ? wid - 729 : wid - 860) * 12288;
    const unsigned long long a = base + off;
    const unsigned left = size > off ? size - off : 0;
    sT = mk_srd((const void*)(((unsigned long long)rfl((unsigned)(a >> 32)) << 32) | rfl((unsigned)a)), rfl(left < 12288 ? left : 12288));
  }
  unsigned tv = (lane & 15) * 128, vhb = blockIdx.x * 4, vsx = (FL_X + (blockIdx.x >> 4)) * 128, vpx = (FL_X + qblk) * 128, vt4 = threadIdx.x * 4, one = 1;
  PIN(vhb); PIN(vsx); PIN(vpx); PIN(one);
  const unsigned cmask = w == 0 ? 0x2522u : 0x2020u;
  unsigned long long tm7, hbm;
  auto hook = [&](auto G_, int T) INL {
    constexpr int g_ = G_;
    if constexpr (g_ == 49) chores(T, cmask, ws, vhb, vsx, vt4, vpx, one);
    if constexpr (g_ == 53) tm7 = is7(T);
    if constexpr (g_ == 55) hbm = hb_unmoved();
    if constexpr (g_ == 56) {
      if (__builtin_expect((hbm & tm7) != 0ull, 0)) {
#pragma unroll 1
        for (unsigned spins = 0; spins < SPIN_MAX && !hb_all_moved(); ++spins) { if ((spins & 3) == 0) heartbeat(ws); __builtin_amdgcn_s_sleep(100); note_issue<253>(ws, vt4); asm volatile("s_waitcnt vmcnt(0)" ::: "memory"); }
      }
    }
    if constexpr (g_ == 61) { touch(tv, sT); tv += 2048; PIN(tv); }
  };
  GAP();
  {
    bf16x8 zero = {}; PIN(zero); set_m0(0); asm volatile("s_nop 1");
#pragma unroll
    for (int i = 0; i < 16; ++i) AMFMA32Z(i * 16, zero, zero);
  }
  GAP();
  dma_grp(c.dA, c.sA, c.soA + 128, c.mAw + 32768);
  WAIT_VL(22, 15); GAP();
  PIN(xa); PIN(xb);
  u32x4 xq[2];
  {
    const u32x4 a = __builtin_bit_cast(u32x4, xa), b = __builtin_bit_cast(u32x4, xb);
    float v[16], s1 = 0.f, q = 0.f;
#pragma unroll
    for (int e = 0; e < 4; ++e) { v[2 * e] = bflo(a[e]); v[2 * e + 1] = bfhi(a[e]); v[8 + 2 * e] = bflo(b[e]); v[9 + 2 * e] = bfhi(b[e]); }
#pragma unroll
    for (int e = 0; e < 16; ++e) s1 += v[e];
    float mean = xsum32(s1) * (1.0f / DM);
    PIN(mean); GAP();
    dma_grp(c.dB, c.sB, c.soB + 2 * 262144, mBw + 2 * 16384);
    GAP();
#pragma unroll
    for (int e = 0; e < 16; ++e) { v[e] -= mean; q += (e < 8 || xc < 24) ? v[e] * v[e] : 0.f; }
    const float rs = __builtin_amdgcn_rsqf(xsum32(q) * (1.0f / DM) + 1e-5f) * (0.0625f * 1.4426950408889634f);
    xq[0] = (u32x4{cvtpk(v[0] * rs, v[1] * rs), cvtpk(v[2] * rs, v[3] * rs), cvtpk(v[4] * rs, v[5] * rs), cvtpk(v[6] * rs, v[7] * rs)});
    xq[1] = (u32x4{cvtpk(v[8] * rs, v[9] * rs), cvtpk(v[10] * rs, v[11] * rs), cvtpk(v[12] * rs, v[13] * rs), cvtpk(v[14] * rs, v[15] * rs)});
    PIN(xq[0]); PIN(xq[1]);
  }
  GAP();
  dma_grp(c.dA, c.sA, c.soA + 128 + AH, c.mAw + 32768 + 4096);
  Frags F0, F1;
  WAIT_VL(18, 15); BAR(); GAP();
#pragma unroll
  for (int n2 = 0; n2 < 8; ++n2) {
    const unsigned ad = c.rB[n2 & 3] + (n2 >> 2) * 4096;
    F0.blo[n2] = ldstr(ad); F0.bhi[n2] = ldstr(ad + 512);
  }
#pragma unroll
  for (int m = 0; m < 8; ++m) F0.a[m] = lds128(c.rA[0] + m * 2048);
  GAP();
  dma_grp(c.dB, c.sB, c.soB + 3 * 262144, mBw + 3 * 16384);
  dma_grp(c.dA, c.sA, c.soA + 256, c.mAw + 65536);
  {
    const srd_t sXo = mk_srd(ws + WS_XN, SEQ * DM * 2);
    const unsigned xo = (xrow >> 5) * 28 * 1024 + (xrow & 31) * 16 + xc * 512;
    st16(xq[0], sXo, xo, 0); st16(xq[1], sXo, xc < 24 ? xo + 16384 : 0x7fffff00u, 0);
  }
  const srd_t sX = mk_srd(ws + WS_XN, SEQ * DM * 2);
  const unsigned soX = (qblk * 4 + w) * 28 * 1024;
  bf16x8 xf[28];
  auto last = [&](auto G_) INL {
    constexpr int g_ = G_;
    if constexpr (g_ == 0) { if (peek_value<255>(y, S_X) < 16u) await(y, S_X, flagp(ws, FL_X + qblk), 16); }
    if constexpr ((g_ & 3) == 1) { constexpr int k = g_ >> 2; vload16s(xf[k], l16, sX, soX + (k >> 2) * 4096, (k & 3) * 1024); }
  };
  if (isv) kv_loop<true>(F0, F1, c, hook, last); else kv_loop<false>(F0, F1, c, hook, last);
  const unsigned stH = isv ? 4096u : 8192u;
  const unsigned o0 = wr * 4 * 131072 + (isv ? mt * 16 + wc * 8 : mt * 16 + wc * 4) * 1024u;
  const srd_t sO = mk_srd(ws + (isv ? WS_V : WS_K) + (size_t)kvhead * (SEQ * HD * 2), SEQ * HD * 2);
  u32x4 pk[4];
  static_for<4>([&](auto N_) INL {
    constexpr int n = N_;
    static_for<3>([&](auto K_) INL { constexpr int k = 16 + 3 * n + K_; vload16s(xf[k], l16, sX, soX + (k >> 2) * 4096, (k & 3) * 1024); });
    pk[n] = pack2(aread(n * 8), aread(n * 8 + 4)); PIN(pk[n]); GAP();
  });
  dma_grp(c.dQ, c.sQ, q_so(9), ldsb + L1_A + w * 4096 + 16384);
#pragma unroll
  for (int n = 0; n < 4; ++n) st16(pk[n], sO, l16, o0 + n * 1024);
  const int h = lane >> 5;
  s16x4 wl[8], wh[8];
  unsigned rQ[2], rQh[2], rQh2[2];
  {
    const unsigned q = (lane >> 2) & 3, p = lane & 3, blk = (lane >> 4) & 1;
#pragma unroll
    for (int cc = 0; cc < 2; ++cc) {
      rQ[cc] = ldsb + h * 1024 + q * 128 + ((cc ^ (q >> 1)) * 64) + blk * 32 + ((p >> 1) | ((p & 1) << 1)) * 8;
      rQh[cc] = rQ[cc] + L1_A; PIN(rQh[cc]);
      rQh2[cc] = rQ[cc] + L1_A + 65536; PIN(rQh2[cc]);
    }
  }
  auto rdq = [&](int f) INL {
    const int ks = (f % 56) >> 1, cc = f & 1, u = (f / 56) * 14 + (ks >> 1), n = u >> 2;
    const unsigned base = n < 4 ? rQ[cc] + n * 16384u : n < 6 ? rQh[cc] + (n - 2) * 16384u : n < 8 ? rQh2[cc] + (n - 6) * 16384u
                        : n < 10 ? rQh[cc] + (n - 8) * 16384u : rQ[cc] + (n - 10) * 16384u;
    const unsigned ad = base + (u & 3) * 4096 + (ks & 1) * 2048;
    wl[f & 7] = ldstr(ad); wh[f & 7] = ldstr(ad + 512);
  };
  WAIT_VL(8 + 12 + 8 + 4, 15); BAR(); GAP();
#pragma unroll
  for (int f = 0; f < QW; ++f) rdq(f);
  GAP();
  float et[8], qt[16]; unsigned ep[4], qp[8];
  auto qmicro = [&](auto G_, auto M_) INL {
    constexpr int db = 2 * G_ + M_ / 29, m = M_ % 29;
    if constexpr (m < 16) { AREAD1(qt[m], db * 16 + m); }
    else if constexpr (m < 24) { constexpr int k = m - 16, c2 = k >> 2, d = k & 3, r0 = (d >> 1) * 8 + c2 * 4 + (d & 1) * 2; qp[k] = cvtpk(qt[r0], qt[r0 + 1]); PIN(qp[k]); }
    else if constexpr (m == 26) { Qo[db][0] = u32x4{qp[0], qp[1], qp[2], qp[3]}; Qo[db][1] = u32x4{qp[4], qp[5], qp[6], qp[7]}; swap16(Qo[db][0], Qo[db][1]); PIN(Qo[db][0]); PIN(Qo[db][1]); }
    else if constexpr (m >= 27) st16g(Qo[db][m - 27], sQs, l16, ((qblk * 8 + w * 2 + (m - 27)) * 8 + db) * 1024);
  };
  static_for<224>([&](auto F_) INL {
    constexpr int f = F_, cq = f / 56, ks = (f % 56) >> 1, db = 2 * cq + (f & 1);
    if constexpr (f == QSIG0) { WAIT_VL(q_ops(21, f), 15); signal_wg(flagp(ws, FL_KV0 + kvhead)); GAP(); }
    if constexpr (f == 160 - QW) { WAIT_VL(q_st(95) + q_ops(96, f), 15); signal_wg(flagp(ws, FL_KV1 + kvhead)); GAP(); }
    if constexpr (f == 160 - QW + 1) { try_k(l16, std::integral_constant<int, q_ops(QPEEK0 + 1, f)>{}); GAP(); }
    if constexpr (f < 56 && (f & 3) == 0) { WAIT_VL(x_younger(ks + 1) + q_ops(0, f), 2 * QW - 4); GAP(); PIN(xf[ks]); PIN(xf[ks + 1]); GAP(); }
    else if constexpr ((f & 1) == 0) { WAIT_VL(63, f + QW <= 224 ? 2 * QW - 4 : 0); }
    if constexpr (ks == 0) AMFMA32Z(db * 16, cat(wl[f & 7], wh[f & 7]), xf[ks]); else AMFMA32(db * 16, cat(wl[f & 7], wh[f & 7]), xf[ks]);
    GAP();
    if constexpr (f + QW < 224) rdq(f + QW);
    if constexpr (f >= 64 && f < 96 && (f & 7) == 0) set_m0(mBw + ((f - 64) >> 3) * 16384);
    if constexpr (q_dma(f)) dma16<((f >> 1) & 3) * 1024>(c.dQ[(f >> 1) & 3], c.sQ, q_so(10 + ((f - 64) >> 3)));
    static_for<kv_lo(f + 1) - kv_lo(f)>([&](auto M_) INL {
      constexpr int m = kv_lo(f) + M_, sn = 4 + m / 13, st = m % 13;
      if constexpr (m < 364) {
        if constexpr (st < 8) { AREAD1(et[st], sn * 8 + st); }
        else if constexpr (st < 12) { ep[st - 8] = cvtpk(et[2 * (st - 8)], et[2 * (st - 8) + 1]); PIN(ep[st - 8]); }
      }
    });
    if constexpr (cq >= 1 && (f + 1) % 56 != 0)
      static_for<qe_lo(f + 1) - qe_lo(f)>([&](auto M_) INL { qmicro(std::integral_constant<int, cq - 1>{}, std::integral_constant<int, qe_lo(f) + M_>{}); });
    if constexpr (f == QPEEK0 || f == QPEEK) peek_issue<255>(flagp(ws, FL_KV0 + head));
    GAP();
    static_for<kv_lo(f + 1) - kv_lo(f)>([&](auto M_) INL {
      constexpr int m = kv_lo(f) + M_, sn = 4 + m / 13;
      if constexpr (m < 364 && m % 13 == 12) st16g(u32x4{ep[0], ep[1], ep[2], ep[3]}, sO, l16, o0 + (sn >> 3) * 131072 + ((sn >> 2) & 1) * stH + (sn & 3) * 1024);
    });
  });
  try_k(l16, std::integral_constant<int, q_ops(QPEEK + 1, 224)>{});
  asm volatile("s_nop 7\n\ts_nop 7");
  static_for<58>([&](auto M_) INL { qmicro(std::integral_constant<int, 3>{}, M_); });
}

constexpr float THR = 8.0f;
DI float max3f(float a, float b, float c) { float r; asm volatile("v_max3_f32 %0, %1, %2, %3" : "=v"(r) : "v"(a), "v"(b), "v"(c)); return r; }
DI float min2f(float a, float b) { float r; asm("v_min_f32 %0, %1, %2" : "=v"(r) : "v"(a), "v"(b)); return r; }
DI float xmax16_32(float x) {
  auto p = __builtin_amdgcn_permlane16_swap(__float_as_uint(x), __float_as_uint(x), false, false);
  x = fmaxf(__uint_as_float(p[0]), __uint_as_float(p[1]));
  auto q = __builtin_amdgcn_permlane32_swap(__float_as_uint(x), __float_as_uint(x), false, false);
  return fmaxf(__uint_as_float(q[0]), __uint_as_float(q[1]));
}
DI unsigned nxt_slot(unsigned s) { return s == 4 * 32768u ? 0u : s + 32768u; }

struct AT {
  bf16x8 Q[8][2];
  f32x4 S[4][2];
  u32x4 P[2][2];
  f32x4 negm[2];
  bf16x8 F[16];
  bf16x8 ones;
  float m[2], f[2], mx;
  unsigned l16;
};
#define EXP1(c, e) do { (c).S[(e) >> 3][((e) >> 2) & 1][(e) & 3] = __builtin_amdgcn_exp2f((c).S[(e) >> 3][((e) >> 2) & 1][(e) & 3]); PIN((c).S[(e) >> 3][((e) >> 2) & 1]); } while (0)
#define PACK1(c, v, qb, d) do { (c).P[v][qb][d] = cvtpk((c).S[2 * (v) + ((d) >> 1)][qb][2 * ((d) & 1)], (c).S[2 * (v) + ((d) >> 1)][qb][2 * ((d) & 1) + 1]); PIN((c).P[v][qb]); } while (0)
constexpr int koff(int f) { return (((f >> 4) * 2 + (f & 1)) * 8 + ((f >> 1) & 7)) * 1024; }
constexpr int voff(int f) { return f * 1024; }

struct DmaT { srd_t srd; unsigned so, step, m0, v[4]; };
template <int G> DI void dma_gap(const AT& c, DmaT& d) {
  if constexpr (G == 17) { d.so += d.step; PINS(d.so); }
  if constexpr (G == 1) set_m0(d.m0);
  if constexpr (G == 17) set_m0(d.m0 + 4096);
  if constexpr ((G & 3) == 2) dma16s<((G >> 2) & 3) * 1024>(d.v[(G >> 2) & 3], d.srd, d.so);
}
template <int T> DI void pack_p1(AT& c) { PACK1(c, 1, (T < 4 ? (T >> 1) : ((T >> 1) & 1)), (T < 4 ? (T & 1) : 2 + (T & 1))); }
template <int T> DI void pack_p0(AT& c) { PACK1(c, 0, (T >> 1) & 1, (T >> 2) * 2 + (T & 1)); }
constexpr int odd_slot(int g) { return (g & 1) && (g & 7) != 7; }
constexpr int odd_rank(int g0, int g) { int n = 0; for (int k = g0; k < g; ++k) n += odd_slot(k); return n; }

template <int g> DI void fin_gap(AT& c) {
  if constexpr (g < 24 && (g & 1) == 0) EXP1(c, 20 + (g >> 1));
  if constexpr (g >= 9 && g < 31 && odd_slot(g)) { constexpr int t = odd_rank(9, g); if constexpr (t < 8) pack_p1<t>(c); }
}

template <class F> DI void phaseA(AT& c, unsigned rd0, unsigned rd1, DmaT d, F&& extra) {
  static_for<64>([&](auto G_) INL {
    constexpr int g = G_, f = g >> 1, qb = g & 1, kb = 2 * (f >> 4) + (f & 1), s = (f >> 1) & 7;
    if constexpr (g == 32) { WAIT_VL(16, 4); BAR(); } else if constexpr ((g & 7) == 0) { WAIT_VL(63, 4); }
    constexpr int qs = qb ^ (f & 1);
    if constexpr (s == 0) SMFMA0(c.S[kb][qs], c.F[f % 16], c.Q[s][qs], c.negm[qs]);
    else SMFMA(c.S[kb][qs], c.F[f % 16], c.Q[s][qs]);
    GAP();
    if constexpr (qb == 1) {
      constexpr int fn = f + 8;
      if constexpr (fn < 32) c.F[fn % 16] = lds128(rd0 + koff(fn)); else c.F[fn % 16] = lds128(rd1 + voff(fn - 32));
    }
    fin_gap<g>(c);
    if constexpr (g >= 32) dma_gap<g - 32>(c, d);
    if constexpr (g >= 41 && odd_slot(g) && g != 49) {
      constexpr int t = odd_rank(41, g) - (g > 49);
      if constexpr (t == 0) c.mx = max3f(c.S[0][0][0], c.S[0][0][1], c.S[0][0][1]);
      else if constexpr (t < 8) c.mx = max3f(c.mx, c.S[t >> 2][(t >> 1) & 1][2 * (t & 1)], c.S[t >> 2][(t >> 1) & 1][2 * (t & 1) + 1]);
    }
    extra(G_);
    GAP();
  });
}

DI void rare_path(AT& c, bool first, bool mask, int kq, bool& pend) {
  float mq[2];
  if (mask) {
    const float u0 = fmaf((float)kq, -1e30f, 5e29f);
    static_for<20>([&](auto C_) INL {
      constexpr int dq = C_ / 4 - 1, i = C_ & 3;
      const float u = u0 - (float)(16 * dq + i) * 1e30f;
      if constexpr (dq >= 0) c.S[dq][0][i] = min2f(c.S[dq][0][i], u);
      if constexpr (dq < 3) c.S[dq + 1][1][i] = min2f(c.S[dq + 1][1][i], u);
    });
  }
#pragma unroll
  for (int qb = 0; qb < 2; ++qb) {
    float t = max3f(c.S[0][qb][0], c.S[0][qb][1], c.S[0][qb][2]), u = max3f(c.S[0][qb][3], c.S[1][qb][0], c.S[1][qb][1]);
    t = max3f(t, c.S[1][qb][2], c.S[1][qb][3]); u = max3f(u, c.S[2][qb][0], c.S[2][qb][1]);
    t = max3f(t, c.S[2][qb][2], c.S[2][qb][3]); u = max3f(u, c.S[3][qb][0], c.S[3][qb][1]);
    t = max3f(t, c.S[3][qb][2], c.S[3][qb][3]);
    mq[qb] = max3f(t, u, u);
  }
  if (__builtin_expect(first || __builtin_amdgcn_ballot_w64(max3f(mq[0], mq[1], mq[1]) > THR) != 0, 0)) {
#pragma unroll
    for (int qb = 0; qb < 2; ++qb) {
      const float mr = xmax16_32(mq[qb]);
      const float dl = first ? (mr > -1e29f ? mr : 0.f) : fmaxf(mr, 0.f);
      c.m[qb] += dl;
      c.f[qb] = __builtin_amdgcn_exp2f(-dl);
#pragma unroll
      for (int kb = 0; kb < 4; ++kb) c.S[kb][qb] -= dl;
      c.negm[qb] = f32x4{-c.m[qb], -c.m[qb], -c.m[qb], -c.m[qb]};
    }
    pend = !first;
  }
}
template <bool DRAIN, class R> DI void phaseB(AT& c, unsigned rd0, unsigned rd1, DmaT d, float thr, R&& rare) {
  float mx2;
  static_for<64>([&](auto G_) INL {
    constexpr int g = G_, f = g >> 1, qb = g & 1, v = f >> 4, db = DRAIN ? (f & 1) * 8 + ((f >> 1) & 7) : f & 15;
    if constexpr (!DRAIN && g == 32) { WAIT_VL(8, 4); BAR(); }
    else if constexpr (g == 32) { WAIT_VL(63, 4); BAR(); }
    else if constexpr ((g & 7) == 0) { if constexpr (DRAIN && g == 56) { WAIT_VL(63, 0); } else { WAIT_VL(63, 4); } }
    if constexpr ((g & 31) == 0) {
      AMFMA(128, c.ones, __builtin_bit_cast(bf16x8, c.P[v][0])); GAP();
      if constexpr (!DRAIN && g == 32) EXP1(c, 11);
      GAP();
      AMFMA(132, c.ones, __builtin_bit_cast(bf16x8, c.P[v][1])); GAP();
      if constexpr (!DRAIN && g == 32) EXP1(c, 12);
      GAP();
    }
    AMFMA(db * 8 + (qb ^ (~f & 1)) * 4, c.F[f % 16], __builtin_bit_cast(bf16x8, c.P[v][qb ^ (~f & 1)]));
    GAP();
    if constexpr (qb == 1) {
      constexpr int fn = f + 8;
      if constexpr (fn < 32) c.F[fn % 16] = lds128(rd0 + (DRAIN ? koff(fn) : voff(fn))); else if constexpr (!DRAIN) c.F[fn % 16] = lds128(rd1 + koff(fn - 32));
    }
    if constexpr (DRAIN) {
      fin_gap<g>(c);
      if constexpr (g >= 32) dma_gap<g - 32>(c, d);
    } else {
      if constexpr (g == 2) { c.mx = max3f(c.mx, c.S[2][0][0], c.S[2][0][1]); mx2 = max3f(c.S[2][0][2], c.S[2][0][3], c.S[2][1][0]); }
      if constexpr (g == 3) c.mx = max3f(c.mx, c.S[2][1][1], c.S[2][1][2]);
      if constexpr (g == 4) { mx2 = max3f(mx2, c.S[2][1][3], c.S[3][0][0]); c.mx = max3f(c.mx, c.S[3][0][1], c.S[3][0][2]); }
      if constexpr (g == 5) mx2 = max3f(mx2, c.S[3][0][3], c.S[3][1][0]);
      if constexpr (g == 6) { c.mx = max3f(c.mx, c.S[3][1][1], c.S[3][1][2]); c.mx = max3f(c.mx, mx2, c.S[3][1][3]); }
      if constexpr (g == 8) {
        if (__builtin_expect(__builtin_amdgcn_ballot_w64(c.mx > thr) != 0, 0)) rare();
      }
      if constexpr (g >= 10 && g < 32 && qb == 0) EXP1(c, (g - 10) >> 1);
      if constexpr (g >= 32 && g < 60 && (g & 3) == 0) EXP1(c, 13 + ((g - 32) >> 2));
      if constexpr (g >= 35 && odd_slot(g) && g != 49) { constexpr int t = odd_rank(35, g) - (g > 49); if constexpr (t < 8) pack_p0<t>(c); }
      if constexpr (g >= 32) dma_gap<g - 32>(c, d);
    }
    GAP();
  });
}

constexpr unsigned L_W2 = 112 * 1024, L_SCR = 156 * 1024;
constexpr int f1_ld(int F) { return (F & 1) && F < 32; }
constexpr int f1_sl(int F) { return F < 59 ? 0 : F >= 89 ? 118 : (F - 59) * 4; }
constexpr int f1_st(int F) { int n = 0; for (int m = f1_sl(F); m < f1_sl(F + 1) && m < 118; ++m) n += m % 59 >= 57; return n; }
constexpr int f1_dma(int F) { return (F >= 41 && F < 57 && (F & 1)) || (F >= 61 && F < 109 && (F & 3) == 1); }
constexpr int f1_ops(int F0, int F1) { int n = 0; for (int F = F0; F < F1; ++F) n += f1_ld(F) + f1_dma(F) + f1_st(F); return n; }
DI void tail(const Sync& sy, const bf16x8 (&xr)[2], const u16* w1, const u16* w2, u16* out, char* ws, unsigned ldsb, unsigned w) {
  unsigned lane_;
  asm volatile("v_mbcnt_lo_u32_b32 %0, -1, 0\n\tv_mbcnt_hi_u32_b32 %0, -1, %0" : "=v"(lane_));
  const int lane = lane_, g = lane >> 4, t = w * 64 + lane;
  const int b = (blockIdx.x & 7) * 2 + ((blockIdx.x >> 3) & 1), i = blockIdx.x >> 4;
  const unsigned l16 = lane * 16;
  const srd_t sW2 = mk_srd(w2, FF * DM * 2);
  unsigned d2[4];
#pragma unroll
  for (int k = 0; k < 4; ++k) d2[k] = (8 * k + (lane >> 3)) * (DM * 2) + (((lane & 7) ^ ((lane >> 4) & 1)) * 16) - k * 1024;
  auto k56 = [&](int s) -> unsigned { const unsigned a = 7 * w + s % 7; return 2 * a - (a & 1) + 2 * (s / 7); };
  const unsigned lA = L_W2 + w * 8192, lB = w * 24576, lC = (24 + w) * 4096;
  auto w2slot = [&](int s) -> unsigned { return s < 4 || s == 7 || s == 8 ? lA + ((s < 4 ? s : s - 7) & 1) * 4096 : s < 7 ? lB + (s - 4) * 8192 : s < 12 ? lB + (s - 9) * 8192 + 4096 : s == 12 ? lC : lB; };
  auto w2so = [&](int s) -> unsigned { return k56(s) * 32 * (DM * 2) + (i >> 1) * 128; };
  auto dma_w2 = [&](int s) INL { dma_grp(d2, sW2, w2so(s), ldsb + w2slot(s)); };
  if (i < 14) { dma_w2(0); dma_w2(1); }
  const srd_t sH = mk_srd(ws + WS_H, SEQ * DM * 2), sHN = mk_srd(ws + WS_HN, SEQ * DM * 2);
  {
    const int row = b * 128 + i * 8 + (t & 7), cg = t >> 3;
    const bool act = cg < 28;
    u32x4 xv[2] = {__builtin_bit_cast(u32x4, xr[0]), __builtin_bit_cast(u32x4, xr[1])};
    const srd_t sY = mk_srd(ws + WS_Y, 8 * SEQ * DM * 2);
    unsigned vo = act ? ((row >> 5) * 56 + cg) * 512 + (row & 31) * 16 : 0x7ffffff0u, ho = act ? row * (DM * 2) + cg * 16 : 0x7ffffff0u, no[2];
#pragma unroll
    for (int c2 = 0; c2 < 2; ++c2) { const int ch = cg + 28 * c2; no[c2] = act ? (row >> 5) * 28 * 1024 + ch * 512 + (row & 31) * 16 : 0x7ffffff0u; PIN(no[c2]); }
    float v[16];
    unsigned cl = 0x00003f80u, ch = 0x3f800000u, c1 = 0x3f803f80u;
    PINS(cl); PINS(ch); PINS(c1); PIN(vo); PIN(ho);
#pragma unroll
    for (int c2 = 0; c2 < 2; ++c2)
#pragma unroll
      for (int e = 0; e < 4; ++e) { v[c2 * 8 + 2 * e] = bflo(xv[c2][e]); v[c2 * 8 + 2 * e + 1] = bfhi(xv[c2][e]); PIN(v[c2 * 8 + 2 * e]); PIN(v[c2 * 8 + 2 * e + 1]); }
    await_wg(sy, S_Y, flagp(ws, FL_Y + b), 16, w);
    u32x4 y[8][2];
#pragma unroll
    for (int p = 0; p < 8; ++p) { y[p][0] = ld16(sY, vo, p * (SEQ * DM * 2)); y[p][1] = ld16(sY, vo + 28 * 512, p * (SEQ * DM * 2)); }
    GAP();
#pragma unroll
    for (int p = 0; p < 8; ++p)
#pragma unroll
      for (int c2 = 0; c2 < 2; ++c2)
#pragma unroll
        for (int e = 0; e < 4; ++e) { const unsigned u = y[p][c2][e]; v[c2 * 8 + 2 * e] = dot2(u, cl, v[c2 * 8 + 2 * e]); v[c2 * 8 + 2 * e + 1] = dot2(u, ch, v[c2 * 8 + 2 * e + 1]); }
    float s1 = 0.f, s2 = 0.f;
#pragma unroll
    for (int c2 = 0; c2 < 2; ++c2)
#pragma unroll
      for (int e = 0; e < 4; ++e) { const unsigned hb = cvtpk(v[c2 * 8 + 2 * e], v[c2 * 8 + 2 * e + 1]); xv[c2][e] = hb; s1 = dot2(hb, c1, s1); s2 = dot2(hb, hb, s2); }
    s1 = xsum8_16_32(s1); s2 = xsum8_16_32(s2);
    LAS f32x2* scr = (LAS f32x2*)(size_t)(ldsb + L_SCR);
    if (lane < 8) scr[w * 8 + lane] = f32x2{s1, s2};
    st16(xv[0], sH, ho, 0); st16(xv[1], sH, ho + 448, 0);
#pragma unroll
    for (int c2 = 0; c2 < 2; ++c2)
#pragma unroll
      for (int e = 0; e < 4; ++e) { v[c2 * 8 + 2 * e] = bflo(xv[c2][e]); v[c2 * 8 + 2 * e + 1] = bfhi(xv[c2][e]); }
    WAIT_L0(); BAR(); GAP();
    const f32x2 q0 = scr[lane & 7], q1 = scr[8 + (lane & 7)], q2 = scr[16 + (lane & 7)], q3 = scr[24 + (lane & 7)];
    const float mean = ((q0[0] + q1[0]) + (q2[0] + q3[0])) * (1.0f / DM);
    const float var = fmaxf(((q0[1] + q1[1]) + (q2[1] + q3[1])) * (1.0f / DM) - mean * mean, 0.f);
    const float rs = __builtin_amdgcn_rsqf(var + 1e-5f) * -1.4426950408889634f, nm = -mean * rs;
#pragma unroll
    for (int c2 = 0; c2 < 2; ++c2) {
      st16(u32x4{cvtpk(fmaf(v[c2 * 8], rs, nm), fmaf(v[c2 * 8 + 1], rs, nm)), cvtpk(fmaf(v[c2 * 8 + 2], rs, nm), fmaf(v[c2 * 8 + 3], rs, nm)),
                 cvtpk(fmaf(v[c2 * 8 + 4], rs, nm), fmaf(v[c2 * 8 + 5], rs, nm)), cvtpk(fmaf(v[c2 * 8 + 6], rs, nm), fmaf(v[c2 * 8 + 7], rs, nm))}, sHN, no[c2], 0);
    }
    WAIT_VL(0, 15);
    signal_wg(flagp(ws, FL_H + b * 4 + (i >> 2)));
  }
  if (i >= 14) return;
  const srd_t sH1 = mk_srd(ws + WS_H1, SEQ * FF * 2);
  const unsigned q_ = (lane >> 2) & 3, p_ = lane & 3;
  unsigned r2a[4], r2b[4], r2c[4];
#pragma unroll
  for (int nb = 0; nb < 4; ++nb) {
    const unsigned r2 = ldsb + (g >> 1) * 1024 + (4 * (g & 1) + q_) * 128 + (((2 * p_ + (nb >> 1)) ^ (q_ >> 1)) * 16) + (nb & 1) * 8;
    r2a[nb] = r2 + lA; r2b[nb] = r2 + lB; r2c[nb] = r2 + lC; PIN(r2a[nb]); PIN(r2b[nb]); PIN(r2c[nb]);
  }
  s16x4 vl[24], vh[24];
  auto rd2 = [&](int f) INL {
    const int nb = f & 3, s = f >> 2, wi = f < 16 ? f : 16 + (f & 7);
    const unsigned ad = s < 4 || s == 7 || s == 8 ? r2a[nb] + ((s < 4 ? s : s - 7) & 1) * 4096u : s < 7 ? r2b[nb] + (s - 4) * 8192u
                      : s < 12 ? r2b[nb] + (s - 9) * 8192u + 4096u : s == 12 ? r2c[nb] : r2b[nb];
    vl[wi] = ldstr(ad); vh[wi] = ldstr(ad + 2048);
  };
#pragma unroll
  for (int f = 0; f < 8; ++f) rd2(f);
  WAIT_L0(); GAP();
  static_for<8>([&](auto F_) INL { PIN(vl[F_]); PIN(vh[F_]); });
  dma_w2(2); dma_w2(3);
  const int orow = b * 128 + (i & 1) * 64 + w * 16 + (lane & 15), ocol = (i >> 1) * 64 + g * 16;
  unsigned soh[4];
#pragma unroll
  for (int rq = 0; rq < 4; ++rq) soh[rq] = (b * 8 + (i & 1) * 4 + ((w + rq) & 3)) * 56 * 1024;
  auto ldh = [&](auto K_) INL {
    constexpr int k = K_, s = k >> 2, rq = k & 3;
    unsigned so = soh[rq] + k56(s) * 1024; PINS(so);
    aload16s(k < 28 ? 112 + 4 * k : 4 * (k - 28), l16, sH1, so, 0);
  };
  bf16x8 hv[2];
  {
    unsigned r1[2], r1h[2];
#pragma unroll
    for (int cc = 0; cc < 2; ++cc) {
      r1[cc] = ldsb + (lane >> 5) * 1024 + q_ * 128 + ((cc ^ (q_ >> 1)) * 64) + (g & 1) * 32 + ((p_ >> 1) | ((p_ & 1) << 1)) * 8;
      r1h[cc] = r1[cc] + 57344; PIN(r1h[cc]);
    }
    await(sy, S_H, flagp(ws, FL_H + b * 4 + w), 4);
    auto ldn = [&](auto K_) INL { constexpr int k = K_; aload16s(4 * k, l16, sHN, ((b * 4 + w) * 28 + (k & ~3)) * 1024, (k & 3) * 1024); };
    static_for<12>([&](auto K_) INL { ldn(K_); });
    s16x4 wl[8], wh[8];
    auto rd1 = [&](int F) INL {
      const int ks = (F % 56) >> 1, n = ks >> 1, cc = F & 1;
      const unsigned ad = (n < 7 ? r1[cc] + n * 8192u : r1h[cc] + (n - 7) * 8192u) + (F / 56) * 4096 + (ks & 1) * 2048;
      wl[F & 7] = ldstr(ad); wh[F & 7] = ldstr(ad + 512);
    };
#pragma unroll
    for (int f = 0; f < 6; ++f) rd1(f);
    GAP();
    f32x16 acc[2][2];
    u32x4 o[2];
    float ex[16]; unsigned op[8];
    auto emicro = [&](auto H_, auto M_) INL {
      constexpr int hf = H_, cc = M_ / 59, m = M_ % 59;
      f32x16& a = acc[hf][cc];
      if constexpr (m < 16) { ex[m] = __builtin_amdgcn_exp2f(a[m]); PIN(ex[m]); }
      else if constexpr (m < 32) { ex[m - 16] = __builtin_amdgcn_rcpf(1.0f + ex[m - 16]); PIN(ex[m - 16]); }
      else if constexpr (m < 48) { ex[m - 32] *= a[m - 32]; PIN(ex[m - 32]); }
      else if constexpr (m < 56) { constexpr int k = m - 48, c2 = k >> 2, d = k & 3, r0 = (d >> 1) * 8 + c2 * 4 + (d & 1) * 2; op[k] = cvtpk(ex[r0], ex[r0 + 1]); PIN(op[k]); }
      else if constexpr (m == 56) { o[0] = u32x4{op[0], op[1], op[2], op[3]}; o[1] = u32x4{op[4], op[5], op[6], op[7]}; swap16(o[0], o[1]); PIN(o[0]); PIN(o[1]); }
      else st16(o[m - 57], sH1, l16, ((b * 8 + w * 2 + (m - 57)) * 56 + i * 4 + 2 * hf + cc) * 1024);
    };
    static_for<112>([&](auto F_) INL {
      constexpr int F = F_, hf = F / 56, ks = (F % 56) >> 1, cc = F & 1;
      if constexpr (F == 60) { BAR(); GAP(); }
      if constexpr (F == 100) { WAIT_VL(f1_ops(89, F), 15); signal_wg(flagp(ws, FL_F + b)); GAP(); }
      if constexpr (F < 56 && (F & 3) == 0) { WAIT_VL(ks < 16 ? 10 : 26 - ks + f1_ops(32, F), 15); GAP(); }
      if constexpr (F == 40) { WAIT_L0(); GAP(); static_for<8>([&](auto K_) INL { PIN(vl[8 + K_]); PIN(vh[8 + K_]); }); GAP(); }
      const bf16x8 wf = cat(wl[F & 7], wh[F & 7]);
      if constexpr (ks == 0) VMFMA32_AZ(acc[hf][cc], wf, 0); else VMFMA32_A(acc[hf][cc], wf, 4 * ks);
      GAP();
      if constexpr (F + 6 < 112) rd1(F + 6);
      if constexpr (f1_ld(F)) ldn(std::integral_constant<int, 12 + (F >> 1)>{});
      if constexpr (F >= 30 && F < 38) rd2(F - 22);
      if constexpr (F == 40 || F == 48) set_m0(ldsb + w2slot(7 + (F - 40) / 8));
      if constexpr (F == 60 || F == 76 || F == 92) set_m0(ldsb + w2slot(4 + (F - 60) / 16));
      if constexpr (f1_dma(F)) {
        constexpr int pc = F < 57 ? (F - 41) / 2 : (F - 61) / 4, sk = F < 57 ? 7 + pc / 4 : 4 + pc / 4;
        dma16<(pc & 3) * 1024>(d2[pc & 3], sW2, w2so(sk));
      }
      static_for<(f1_sl(F + 1) < 118 ? f1_sl(F + 1) : 118) - (f1_sl(F) < 118 ? f1_sl(F) : 118)>([&](auto M_) INL { emicro(std::integral_constant<int, 0>{}, std::integral_constant<int, f1_sl(F) + M_>{}); });
      GAP();
    });
    peek_issue<255>(flagp(ws, FL_F + b));
    asm volatile("s_nop 7\n\ts_nop 7");
    PIN(acc[1][0]); PIN(acc[1][1]);
    static_for<118>([&](auto M_) INL { emicro(std::integral_constant<int, 1>{}, M_); });
    WAIT_VL(4, 15); GAP();
    if (peek_value<255>(sy, S_F0) < 14u) await(sy, S_F0, flagp(ws, FL_F + b), 14);
    asm_ld(hv[0], orow * (DM * 2) + ocol * 2, sH, 0); asm_ld(hv[1], orow * (DM * 2) + ocol * 2 + 16, sH, 0);
    static_for<12>([&](auto K_) INL { ldh(K_); });
    WAIT_VL(14, 15);
    signal_wg(flagp(ws, FL_F + 16 + b));
    GAP();
    static_for<8>([&](auto K_) INL { ldh(std::integral_constant<int, 12 + K_>{}); });
  }
  {
    f32x4 acc[4][4];
    static_for<56>([&](auto F_) INL {
      constexpr int f = F_, s = f >> 2, nb = f & 3, wi = f < 16 ? f : 16 + (f & 7), ab = s < 7 ? 112 + 16 * s : 16 * (s - 7);
      if constexpr (nb == 0 && s != 6) { constexpr int wt[14] = {16, 20, 24, 24, 24, 0, 0, 14, 12, 12, 12, 8, 4, 0}; WAIT_VL(wt[s], 15); GAP(); }
      if constexpr (f == 20) { if (peek_value<255>(sy, S_F1) < 14u) await(sy, S_F1, flagp(ws, FL_F + 16 + b), 14); GAP(); }
      const bf16x8 wf = cat(vl[wi], vh[wi]);
      static_for<4>([&](auto R_) INL {
        constexpr int rq = R_;
        if constexpr (s == 0) SMFMA_AZ(acc[nb][rq], wf, ab + 4 * rq); else SMFMA_A(acc[nb][rq], wf, ab + 4 * rq);
        GAP();
        if constexpr (rq == 0 && f + 4 >= 16 && f + 4 < 56) rd2(f + 4);
        if constexpr ((s < 4 || s == 5) && rq == 1 && nb == 0) set_m0(ldsb + w2slot(s < 4 ? 9 + s : 13));
        if constexpr ((s < 4 || s == 5) && rq == 2) dma16<nb * 1024>(d2[nb], sW2, w2so(s < 4 ? 9 + s : 13));
        if constexpr (f < 8 && rq == 0) ldh(std::integral_constant<int, 20 + f>{});
        if constexpr (f == 16 && rq == 2) peek_issue<255>(flagp(ws, FL_F + 16 + b));
        if constexpr (f >= 20 && f < 28 && (rq == 0 || rq == 3)) ldh(std::integral_constant<int, 28 + 2 * (f - 20) + (rq == 3)>{});
        if constexpr (f >= 28 && f < 40 && rq == 2) ldh(std::integral_constant<int, f + 16>{});
        GAP();
      });
    });
    asm volatile("s_nop 7");
    static_for<16>([&](auto F_) INL { PIN(acc[F_ >> 2][F_ & 3]); });
    BAR(); GAP();
#pragma unroll
    for (int rq = 1; rq < 4; ++rq)
#pragma unroll
      for (int nb = 0; nb < 4; ++nb) *(LAS f32x4*)(size_t)(ldsb + ((w + rq) & 3) * 12288 + ((rq - 1) * 4 + nb) * 1024 + l16) = acc[nb][rq];
    WAIT_L0(); BAR(); GAP();
    f32x4 o[4];
#pragma unroll
    for (int nb = 0; nb < 4; ++nb) {
      const unsigned ad = ldsb + w * 12288 + nb * 1024 + l16;
      o[nb] = ((acc[nb][0] + *(const LAS f32x4*)(size_t)ad) + (*(const LAS f32x4*)(size_t)(ad + 4096) + *(const LAS f32x4*)(size_t)(ad + 8192))) * -0.6931471805599453f;
    }
    PIN(hv[0]); PIN(hv[1]);
    const u32x4 h0 = __builtin_bit_cast(u32x4, hv[0]), h1 = __builtin_bit_cast(u32x4, hv[1]);
    u32x4* op = (u32x4*)(out + (size_t)orow * DM + ocol);
    op[0] = pack2(o[0] + f32x4{bflo(h0[0]), bfhi(h0[0]), bflo(h0[1]), bfhi(h0[1])}, o[1] + f32x4{bflo(h0[2]), bfhi(h0[2]), bflo(h0[3]), bfhi(h0[3])});
    op[1] = pack2(o[2] + f32x4{bflo(h1[0]), bfhi(h1[0]), bflo(h1[1]), bfhi(h1[1])}, o[3] + f32x4{bflo(h1[2]), bfhi(h1[2]), bflo(h1[3]), bfhi(h1[3])});
  }
}

__global__ __launch_bounds__(256, 1) __attribute__((amdgpu_num_vgpr(248))) void k1_block(const u16* __restrict__ x, const u16* __restrict__ ckv, const u16* __restrict__ wq,
                                                    const u16* __restrict__ wuk, const u16* __restrict__ wuv, const u16* __restrict__ wo,
                                                    const u16* __restrict__ w1, const u16* __restrict__ w2, u16* __restrict__ out, char* __restrict__ ws) {
  extern __shared__ __attribute__((aligned(16))) char lds[];
  const unsigned ldsb = (unsigned)(size_t)(LAS char*)lds;
  const int xcd = blockIdx.x & 7, ib = blockIdx.x >> 3;
  const int head = 2 * xcd + (ib & 1), j = (ib >> 1) & 7;
  const bool isB = ib >> 4;
  const int n1 = isB ? 15 - 2 * j : 64, t0 = isB ? 0 : 16, nrun = isB ? 15 - 2 * j : 16 - 2 * j, twrap = isB ? 0 : 15 - 2 * j;
  const unsigned w = rfl(threadIdx.x >> 6);
  const srd_t sQ = mk_srd(ws + WS_Q + (size_t)head * (SEQ * HD * 2), SEQ * HD * 2);
  const srd_t sK = mk_srd(ws + WS_K + (size_t)head * (SEQ * HD * 2), SEQ * HD * 2);
  const srd_t sV = mk_srd(ws + WS_V + (size_t)head * (SEQ * HD * 2), SEQ * HD * 2);
  const unsigned mw = ldsb + w * 8192;
  auto tile_so = [&](int i) -> unsigned { return (unsigned)(i < nrun ? t0 + i : twrap + i - nrun) * 32768u + w * 8192; };
  auto dma_tile = [&](unsigned l16, srd_t s, unsigned so, unsigned slot) INL {
    set_m0(mw + slot); asm volatile("s_nop 0");
    dma16s<0>(l16, s, so); dma16s<1024>(l16, s, so); dma16s<2048>(l16, s, so); dma16s<3072>(l16, s, so);
    set_m0(mw + slot + 4096); asm volatile("s_nop 0");
    dma16s<0>(l16, s, so + 4096); dma16s<1024>(l16, s, so + 4096); dma16s<2048>(l16, s, so + 4096); dma16s<3072>(l16, s, so + 4096);
  };
  AT c;
  int blk = 15 - j;
  Sync y; y.ws = ws;
  const int tb = (blockIdx.x & 7) * 2 + ((blockIdx.x >> 3) & 1);
  const unsigned sidx[S_N] = {FL_X + (isB ? 15u - j : (unsigned)j), FL_KV0 + (unsigned)head, FL_KV1 + (unsigned)head, FL_KV + (unsigned)head, FL_Q + (head * 16 + 15 - j) * 4 + w, FL_Q + (head * 16 + j) * 4 + w,
                              FL_P + (head * 8 + j) * 4 + w, FL_C + ((head ^ 1) * 16 + (isB ? j : 15 - j)) * 4 + w, FL_Y + (unsigned)tb, FL_H + tb * 4 + w, FL_F + (unsigned)tb, FL_F + 16 + (unsigned)tb};
  {
    const int qblk = isB ? 15 - j : j;
    u32x4 Qo[8][2];
    bool early = false, late = true;
    phase1(y, sidx, x, ckv, wq, (ib & 1) ? wuv : wuk, wo, w1, w2, ws, ldsb, w, ib & 1, 2 * xcd + ((ib >> 1) & 1), ib >> 2, head, qblk,
           mk_srd(ws + WS_Q + (size_t)head * (SEQ * HD * 2), isB || j >= 6 ? SEQ * HD * 2 : 0), Qo, [&](unsigned l16, auto younger) INL {
      late = !early;
      if (early) return;
      WAIT_VL(decltype(younger)::value, 15); GAP();
      if (peek_value<255>(y, S_KV0) >= 16u) {
        early = true; dma_tile(l16, sK, tile_so(0), 3 * 32768); dma_tile(l16, sK, tile_so(1), 4 * 32768);
      }
    });
    c.l16 = (threadIdx.x & 63) * 16;
    if (!early) {
      await(y, S_KV0, flagp(ws, FL_KV0 + head), 16);
      dma_tile(c.l16, sK, tile_so(0), 3 * 32768);
      dma_tile(c.l16, sK, tile_so(1), 4 * 32768);
      WAIT_VL(0, 15);
    } else if (late) {
      WAIT_VL(4, 15);
    } else {
      WAIT_VL(q_ops(160 - QW + 1, 224) + 5, 15);
    }
    BAR(); GAP();
    const srd_t sQl = mk_srd(ws + WS_Q + (size_t)head * (SEQ * HD * 2), j >= 6 ? 0 : SEQ * HD * 2);
    if (!isB) {
      if (j >= 6) { WAIT_VL(0, 15); signal(flagp(ws, FL_Q + (head * 16 + qblk) * 4 + w)); }
      await(y, S_Q1, flagp(ws, FL_Q + (head * 16 + blk) * 4 + w), 1);
      static_for<16>([&](auto I_) INL { constexpr int s = I_ >> 1, qb = I_ & 1; asm_ld(c.Q[s][qb], c.l16, sQ, ((blk * 8 + w * 2 + qb) * 8 + s) * 1024); });
#pragma unroll
      for (int k = 0; k < 16; ++k) st16(Qo[k >> 1][k & 1], sQl, c.l16, ((qblk * 8 + w * 2 + (k & 1)) * 8 + (k >> 1)) * 1024);
      WAIT_VL(16, 15); GAP();
      static_for<8>([&](auto S_) INL { PIN(c.Q[S_][0]); PIN(c.Q[S_][1]); });
      GAP();
    } else {
#pragma unroll
      for (int s = 0; s < 8; ++s) { c.Q[s][0] = __builtin_bit_cast(bf16x8, Qo[s][0]); c.Q[s][1] = __builtin_bit_cast(bf16x8, Qo[s][1]); }
    }
  }
  const int lane = threadIdx.x & 63, r = lane & 15, g = lane >> 4;
  const unsigned rdb = ldsb + c.l16;
#pragma unroll
  for (int f = 0; f < 8; ++f) c.F[f] = lds128(rdb + 3 * 32768 + koff(f));
  {
    bf16x8 zero = {}; PIN(zero); asm volatile("s_nop 1");
#pragma unroll
    for (int i = 0; i < 8; ++i) AMFMA32Z(i * 16, zero, zero);
    AZERO4(128); AZERO4(132);
  }
  c.ones = bf16x8{0x3f80, 0x3f80, 0x3f80, 0x3f80, 0x3f80, 0x3f80, 0x3f80, 0x3f80};
  PIN(c.ones);
  c.m[0] = c.m[1] = 0.f; c.f[0] = c.f[1] = 1.f;
  c.negm[0] = f32x4{}; c.negm[1] = f32x4{};
  PIN(c.negm[0]); PIN(c.negm[1]);
  const int kql = 4 * g - r - 32 * (int)w;
  bool pend = false;
  static_for<8>([&](auto S_) INL { PIN(c.Q[S_][0]); PIN(c.Q[S_][1]); });
  WAIT_L0(); GAP();
  unsigned* q2flag = flagp(ws, FL_Q + (head * 16 + j) * 4 + w);
  bool q2todo = isB;
  auto load_q2 = [&]() INL {
    const unsigned so2 = (j * 8 + w * 2) * 8192;
    static_for<16>([&](auto I_) INL {
      constexpr int k = I_;
      unsigned so = so2 + (k >> 2) * 4096; PINS(so);
      if constexpr ((k & 3) == 0) asm volatile("s_nop 4");
      aload16s(136 + 4 * k, c.l16, sQ, so, (k & 3) * 1024);
    });
  };
  {
    unsigned so0[3] = {tile_so(0), tile_so(2), tile_so(1)};
    PINS(so0[0]); PINS(so0[1]); PINS(so0[2]);
    set_m0(mw); GAP();

    static_for<64>([&](auto G_) INL {
      constexpr int g_ = G_, f = g_ >> 1, qb = g_ & 1, kb = 2 * (f >> 4) + (f & 1), s = (f >> 1) & 7;
      if constexpr ((g_ & 7) == 0) { WAIT_VL(63, 4); }
      if constexpr (g_ == 12) { if (isB) { WAIT_VL(6, 15); signal(flagp(ws, FL_Q + (head * 16 + blk) * 4 + w)); } GAP(); }
      if constexpr (g_ == 50) { peek_issue<254>(flagp(ws, FL_KV1 + head)); GAP(); }
      if constexpr (s == 0) SMFMAZ(c.S[kb][qb], c.F[f % 16], c.Q[s][qb]);
      else SMFMA(c.S[kb][qb], c.F[f % 16], c.Q[s][qb]);
      GAP();
      if constexpr (qb == 1) {
        constexpr int fn = f + 8;
        if constexpr (fn < 32) c.F[fn % 16] = lds128(rdb + 3 * 32768 + koff(fn)); else c.F[fn % 16] = lds128(rdb + 4 * 32768 + koff(fn - 32));
      }
      if constexpr (g_ < 48) {
        constexpr int pc = g_ >> 1, tn = pc >> 3;
        if constexpr ((g_ & 7) == 7 && g_ < 47) set_m0(mw + ((pc + 1) >> 3) * 32768 + (((pc + 1) >> 2) & 1) * 4096);
        if constexpr (qb == 0) dma16s<(pc & 3) * 1024>(c.l16, tn == 1 ? sK : sV, so0[tn] + ((pc >> 2) & 1) * 4096);
      }
      GAP();
    });
    asm volatile("s_nop 7");
    PIN(c.S[0][0]); PIN(c.S[0][1]); PIN(c.S[1][0]); PIN(c.S[1][1]); PIN(c.S[2][0]); PIN(c.S[2][1]); PIN(c.S[3][0]); PIN(c.S[3][1]);
    rare_path(c, true, t0 >= 2 * blk + (int)(w >> 1), t0 * 64 - blk * 128 + kql, pend);
    static_for<20>([&](auto E_) INL { constexpr int e = E_; EXP1(c, e); });
    static_for<8>([&](auto T_) INL { pack_p0<T_>(c); });
    GAP();
  }
  WAIT_VL(0, 15); GAP();
  if (!isB && j < 6) signal(q2flag);
  signal(flagp(ws, FL_KV + 2 * xcd + ((ib >> 1) & 1)));
  if (peek_value<254>(y, S_KV1) < 16u) await(y, S_KV1, flagp(ws, FL_KV1 + head), 16);
  const srd_t sP = mk_srd(ws + WS_PART + (size_t)((head * 8 + j) * 4 + w) * PART_BYTES, PART_BYTES);
  unsigned* pflag = flagp(ws, FL_P + (head * 8 + j) * 4 + w);
  const srd_t sWo = mk_srd(wo, HDT * DM * 2);
  const int odd = head & 1;
  unsigned dO[4];
#pragma unroll
  for (int k = 0; k < 4; ++k) dO[k] = (8 * k + (lane >> 3)) * (DM * 2) + (((lane & 7) ^ (((lane >> 4) & 1) << 2)) * 16) - k * 1024;
  auto wo_so = [&](int k) -> unsigned {
    const int lg = k < 4 ? k : (k - 3) & 3, grp = lg == 0 ? 3 : odd * 4 + lg - 1;
    return ((k < 4 ? head : head ^ 1) * 256 + 64 * w) * (DM * 2) + grp * 128;
  };
  unsigned sl0 = 4 * 32768, sl1 = 0, rdA = rdb + 4 * 32768, rdB = rdb, soV = tile_so(2);
  DmaT dK{sK, tile_so(3), 4096, mw + 3 * 32768, {c.l16, c.l16, c.l16, c.l16}}, dV{sV, 0, 4096, 0, {c.l16, c.l16, c.l16, c.l16}};
  int tl = 1 < nrun ? t0 + 1 : twrap + 1 - nrun, tk = 3 < nrun ? t0 + 3 : twrap + 3 - nrun;
  int tslow = 2 * blk + (int)(w >> 1);
  float msave[2] = {0.f, 0.f};
  const int ilw = nrun - 1, ikw = nrun - 3;
  unsigned rdn = rdb;
  auto start_second = [&](int i) INL {
    if (i == n1) {
      if (__builtin_expect(q2todo, 0)) { await(y, S_Q2, q2flag, 1); load_q2(); q2todo = false; asm volatile("s_waitcnt vmcnt(0)" ::: "memory"); }
      else asm volatile("s_waitcnt vmcnt(16)" ::: "memory");
      static_for<64>([&](auto I_) INL {
        constexpr int e = I_, qb = e >> 5, s = (e >> 2) & 7, k = e & 3;
        u32x4 t = __builtin_bit_cast(u32x4, c.Q[s][qb]); unsigned u; AREAD1(u, 136 + e); t[k] = u; c.Q[s][qb] = __builtin_bit_cast(bf16x8, t);
      });
      msave[0] = c.m[0]; msave[1] = c.m[1]; c.m[0] = c.m[1] = 0.f;
      c.negm[0] = f32x4{}; c.negm[1] = f32x4{};
      blk = j; tslow = 0;
      asm volatile("s_nop 1"); PIN(c.negm[0]); PIN(c.negm[1]); GAP();
    }
  };
  auto between = [&](int i) INL {
    if (i == n1 + 1) { asm volatile("s_waitcnt vmcnt(16)" ::: "memory"); signal(pflag); }
    if (i >= 14) {
    if (i < 16) {
      dK.srd = sWo; dK.step = 32 * DM * 2; dK.so = wo_so(i - 14);
#pragma unroll
      for (int k = 0; k < 4; ++k) vset(dK.v[k], dO[k]);
      if (i == 15) {
        peek_issue<255>(pflag);
        rdn = rdb + 32768;
        dV.srd = sWo; dV.step = 32 * DM * 2; soV = wo_so(2);
#pragma unroll
        for (int k = 0; k < 4; ++k) vset(dV.v[k], dO[k]);
      }
    }
    } else if (i == 2) {
      peek_issue<254>(flagp(ws, FL_KV + head)); peek_issue<255>(q2flag);
    } else if (i == 3 || (q2todo && i > 3)) {
      asm volatile("s_waitcnt vmcnt(16)" ::: "memory");
      if (i == 3 && __builtin_expect(peek_value<254>(y, S_KV) < 64u, 0)) await(y, S_KV, flagp(ws, FL_KV + head), 64);
      if (q2todo && i + 1 != n1) { if (__builtin_expect(peek_value<255>(y, S_Q2) != 0u, 1)) { load_q2(); q2todo = false; } else peek_issue<255>(q2flag); }
    }
    if (__builtin_expect(pend || i == n1 || i + 1 == n1, 0)) {
    if (pend) {
      asm volatile("s_nop 7");
      static_for<136>([&](auto I_) INL { constexpr int e = I_; float t; AREAD1(t, e); t *= c.f[(e >> 2) & 1]; AWRITE1(e, t); });
      asm volatile("s_nop 1");
      pend = false;
    }
      if (i == n1) tslow = 2 * blk + (int)(w >> 1);
    if (i == n1) {
      asm volatile("s_nop 7");
      float l0, l1; AREAD1(l0, 128); AREAD1(l1, 132);
      bf16x8 zero = {}; PIN(zero);
#pragma unroll
      for (int u = 0; u < 8; ++u)
#pragma unroll
        for (int qb = 0; qb < 2; ++qb) {
          st16(pack2(aread(2 * u * 8 + qb * 4), aread((2 * u + 1) * 8 + qb * 4)), sP, c.l16, (qb * 8 + u) * 1024);
          if (qb == 1) AMFMA32Z(u * 16, zero, zero);
        }
      const f32x4 ml = {msave[0], l0, msave[1], l1};
      st16(__builtin_bit_cast(u32x4, ml), sP, c.l16, 16384);
      AZERO4(128); AZERO4(132);
    }
      start_second(i + 1);
    }
  };
  const unsigned ev0 = 0xc00cu | (isB ? 7u << (n1 - 1) : 0u);
  unsigned ev = q2todo ? ev0 | ~7u : ev0;
  start_second(1);
#pragma unroll 1
  for (int i = 1; i <= 16; ++i) {
    unsigned thru, soVn, sl2n, sl1n;
    const int tc = tl;
    const DmaT dKc = dK;
    phaseA(c, rdA, rdB, dKc, [&](auto G_) INL {
      constexpr int g_ = G_;
      if constexpr (g_ == 36) { thru = tl >= tslow ? 0xff7fffffu : __float_as_uint(THR); PINS(thru); }
      if constexpr (g_ == 40) { sl2n = nxt_slot(sl1); PINS(sl2n); rdA = rdn + sl2n; PIN(rdA); }
      if constexpr (g_ == 44) { sl1n = nxt_slot(sl2n); PINS(sl1n); }
      if constexpr (g_ == 48) { tl = i == ilw ? twrap : tl + 1; PINS(tl); }
      if constexpr (g_ == 52) { tk = i == ikw ? twrap : tk + 1; PINS(tk); }
      if constexpr (g_ == 56) { soVn = dKc.so; dK.so = ((unsigned)tk << 15) + w * 8192; PINS(dK.so); }
      if constexpr (g_ == 60) { dK.m0 = mw + sl1; PINS(dK.m0); }
    });
    dV.so = soV; dV.m0 = mw + sl0;
    phaseB<false>(c, rdB, rdA, dV, __uint_as_float(thru), [&]() INL { rare_path(c, i == n1, tc >= 2 * blk + (int)(w >> 1), tc * 64 - blk * 128 + kql, pend); });
    soV = soVn; sl0 = sl2n; sl1 = sl1n; rdB = rdb + sl1;
    if (__builtin_expect(pend || ((ev >> i) & 1), 0)) { between(i); ev = q2todo ? ev0 | ~7u : ev0; }
  }
  auto dma_wo = [&](int k, unsigned slot) INL { dma_grp(dO, sWo, wo_so(k), mw + slot); dma_grp(dO, sWo, wo_so(k) + 32 * DM * 2, mw + slot + 4096); };
  u32x4 X[8][2], X2[8][2];
  bf16x8 xr[2];
  const srd_t sC = mk_srd(ws + WS_CTX, SEQ * HDT * 2);
  {
    bf16x8 pb[17];
    if (!isB && peek_value<255>(y, S_P) == 0u) await(y, S_P, pflag, 1);
    const srd_t sPl = mk_srd(ws + WS_PART + (size_t)((head * 8 + j) * 4 + w) * PART_BYTES, isB ? 0 : PART_BYTES);
    static_for<17>([&](auto K_) INL { asm_ld(pb[K_], c.l16, sPl, K_ * 1024u); });
    GAP();
    dV.so = wo_so(3); dV.m0 = mw;
    phaseB<true>(c, rdb + 2 * 32768, 0, dV, 0.f, []() INL {});
    WAIT_VL(8, 15); GAP();
    static_for<17>([&](auto K_) INL { PIN(pb[K_]); });
    float l[2]; AREAD1(l[0], 128); AREAD1(l[1], 132);
    const f32x4 ml = __builtin_bit_cast(f32x4, pb[16]);
    float ca[2], cb[2];
#pragma unroll
    for (int qb = 0; qb < 2; ++qb) {
      const float mb = isB ? -1e30f : ml[2 * qb], lb = ml[2 * qb + 1];
      const float mm = fmaxf(c.m[qb], mb), ea = __builtin_amdgcn_exp2f(c.m[qb] - mm), eb = __builtin_amdgcn_exp2f(mb - mm);
      const float inv = __builtin_amdgcn_rcpf(l[qb] * ea + lb * eb);
      ca[qb] = ea * inv; cb[qb] = eb * inv;
    }
    auto finish = [&](auto MERGE_) INL {
#pragma unroll
      for (int u = 0; u < 8; ++u) {
#pragma unroll
        for (int qb = 0; qb < 2; ++qb) {
          const u32x4 t = __builtin_bit_cast(u32x4, pb[qb * 8 + u]);
          f32x4 o0 = aread(2 * u * 8 + qb * 4) * ca[qb], o1 = aread((2 * u + 1) * 8 + qb * 4) * ca[qb];
          if constexpr (MERGE_) {
            o0 += f32x4{bflo(t[0]), bfhi(t[0]), bflo(t[1]), bfhi(t[1])} * cb[qb];
            o1 += f32x4{bflo(t[2]), bfhi(t[2]), bflo(t[3]), bfhi(t[3])} * cb[qb];
          }
          X[u][qb] = pack2(o0, o1);
          st16(X[u][qb], sC, c.l16, ((blk * 8 + w * 2 + qb) * 128 + head * 8 + u) * 1024);
        }
        swap16(X[u][0], X[u][1]);
      }
    };
    if (isB) finish(std::false_type{}); else finish(std::true_type{});
  }
  {
    const int h = lane >> 5;
    const Sync sy = y;
    unsigned rW[2], rWh[2], rWv[2], rH[3];
    {
      const unsigned q = (lane >> 2) & 3, p = lane & 3, bk = (lane >> 4) & 1;
#pragma unroll
      for (int cc = 0; cc < 2; ++cc) {
        rW[cc] = ldsb + h * 1024 + q * 128 + ((cc ^ (q >> 1)) * 64) + bk * 32 + ((p >> 1) | ((p & 1) << 1)) * 8;
        rWh[cc] = rW[cc] + 65536; PIN(rWh[cc]);
        rWv[cc] = rW[cc] + 131072; PIN(rWv[cc]);
      }
      rH[0] = (odd ? rW[1] : rW[0]) + 32768; rH[1] = rH[0] + 3 * 32768; PIN(rH[0]); PIN(rH[1]);
    }
    WAIT_VL(24, 15); BAR(); GAP();
    s16x4 wl[8], wh[8];
    auto yk = [](int y) constexpr { return y < 16 ? 0 : y < 208 ? 1 + (y - 16) / 32 : 7; };
    auto yt = [](int y) constexpr { return y < 16 ? y : y < 208 ? ((y - 16) & 31) >> 1 : y - 208; };
    auto rdw = [&](auto Y_) INL {
      constexpr int y = Y_, k = yk(y), t = yt(y), cc = (y - 16) & 1;
      constexpr int slot = k == 1 ? 3 : k == 2 ? 4 : k == 3 ? 0 : k == 4 ? 1 : k == 5 ? 2 : 3;
      const unsigned base = k == 0 ? rH[0] : k == 7 ? rH[1] : slot < 2 ? rW[cc] + slot * 32768u : slot < 4 ? rWh[cc] + (slot - 2) * 32768u : rWv[cc];
      const unsigned ad = base + (t >> 1) * 4096 + (t & 1) * 512;
      wl[y & 7] = ldstr(ad); wh[y & 7] = ldstr(ad + 2048);
    };
    static_for<QW>([&](auto Y_) INL { rdw(Y_); });
    GAP();
    const srd_t sY = mk_srd(ws + WS_Y + ((size_t)xcd * 64 + blk * 4 + w) * (56 * 512), 56 * 512);
    const unsigned yo0 = (6 + odd) * 2048, yo1 = odd * 8 * 2048;
    float et[8]; unsigned ep[4];
    auto ymicro = [&](auto M_) INL {
      constexpr int m = M_, sn = m / 13, st = m % 13, mb = sn < 12 ? 1 + (sn >> 1) : 0, c2 = sn & 1;
      if constexpr (st < 8) { AREAD1(et[st], mb * 16 + c2 * 8 + st); }
      else if constexpr (st < 12) { ep[st - 8] = cvtpk(et[2 * (st - 8)], et[2 * (st - 8) + 1]); PIN(ep[st - 8]); }
      else st16(u32x4{ep[0], ep[1], ep[2], ep[3]}, sY, c.l16, (mb == 0 ? yo0 : yo1 + (mb - 1) * 2048) + c2 * 1024);
    };
    const srd_t sW1 = mk_srd(w1, (blockIdx.x >> 4) < 14 ? DM * FF * 2 : 0);
    unsigned d1[4];
#pragma unroll
    for (int k = 0; k < 4; ++k) d1[k] = (8 * k + (lane >> 3)) * (FF * 2) + (((lane & 7) ^ (((lane >> 4) & 1) << 2)) * 16) - k * 1024;
    const unsigned so1 = (w >> 1) * 32 * (FF * 2) + (blockIdx.x >> 4) * 256 + (w & 1) * 128;
    static_for<224>([&](auto Y_) INL {
      constexpr int y = Y_, k = yk(y), t = yt(y), cc = (y - 16) & 1;
      constexpr int mb = (k == 0 || k == 7) ? 0 : 1 + 2 * ((k - 1) % 3) + cc;
      if constexpr (y == 24) { WAIT_VL(8, 15); signal(flagp(ws, FL_C + (head * 16 + blk) * 4 + w)); GAP(); }
      if constexpr (y == 64) {
        WAIT_VL(8, 15); GAP();
        if (peek_value<255>(sy, S_C) == 0u) await(sy, S_C, flagp(ws, FL_C + ((head ^ 1) * 16 + blk) * 4 + w), 1);
        GAP();
      }
      if constexpr (y == 48 || y == 144 || y == 176) { BAR(); GAP(); }
      if constexpr (y == 112 - QW) { WAIT_VL(16 + (112 - QW - 96) / 2, 15); BAR(); GAP(); }
      if constexpr (y >= 110 && y < 126 && (y & 1) == 0) {
        constexpr int u = (y - 110) >> 1;
        if constexpr (y == 110) { WAIT_VL(7, 15); GAP(); static_for<8>([&](auto U_) INL { PIN(X2[U_][0]); PIN(X2[U_][1]); }); }
        swap16(X2[u][0], X2[u][1]); PIN(X2[u][0]); PIN(X2[u][1]); GAP();
      }
      if constexpr (y == 176 - QW) { WAIT_VL((2 * (176 - QW - 146)) / 13 + (176 - QW - 144) / 2, 15); BAR(); GAP(); }
      const bf16x8 bop = __builtin_bit_cast(bf16x8, k < 4 ? X[t >> 1][t & 1] : X2[t >> 1][t & 1]);
      if constexpr (k < 4 && t == 0) AMFMA32Z(mb * 16, cat(wl[y & 7], wh[y & 7]), bop); else AMFMA32(mb * 16, cat(wl[y & 7], wh[y & 7]), bop);
      GAP();
      if constexpr (y + QW < 224) rdw(std::integral_constant<int, y + QW>{});
      if constexpr (y == 132) {
        const unsigned xo = threadIdx.x < 224 ? (tb * 128 + (blockIdx.x >> 4) * 8 + (lane & 7)) * (DM * 2) + (threadIdx.x >> 3) * 16 : 0x7ffff000u;
        vload16(xr[0], xo, mk_srd(x, SEQ * DM * 2), 0, 0); vload16(xr[1], xo, mk_srd(x, SEQ * DM * 2), 0, 448);
      }
      if constexpr (y == 46) peek_issue<255>(flagp(ws, FL_C + ((head ^ 1) * 16 + blk) * 4 + w));
      if constexpr (y >= 64 && y < 96 && (y & 1) == 0) {
        constexpr int u = (y - 64) >> 2, qb = ((y - 64) >> 1) & 1;
        bf16x8 tv; asm_ld(tv, c.l16, sC, ((blk * 8 + w * 2 + qb) * 128 + (head ^ 1) * 8 + u) * 1024); X2[u][qb] = __builtin_bit_cast(u32x4, tv);
      }
      if constexpr (y < 16 || (y >= 48 && y < 64) || (y >= 96 && y < 128)) {
        constexpr int pc = (y & 15) >> 1, sk = y < 16 ? 5 : y < 64 ? 4 : y < 112 ? 6 : 7, sslot = y < 16 ? 2 : y < 64 ? 1 : y < 112 ? 3 : 4;
        if constexpr ((y & 7) == 0) set_m0(mw + sslot * 32768 + (pc >> 2) * 4096);
        if constexpr (y & 1) dma16<(pc & 3) * 1024>(dO[pc & 3], sWo, wo_so(sk) + (pc >> 2) * 32 * DM * 2);
      }
      if constexpr (y >= 144 && y < 208) {
        constexpr int per = y < 176 ? 2 : 4, pc = y < 176 ? (y - 144) / 2 : 16 + (y - 176) / 4, rd = pc >> 2;
        if constexpr ((pc & 3) == 0 && y % per == 0) set_m0(ldsb + w * 4096 + rd * 16384);
        if constexpr (y % per == 1) dma16<(pc & 3) * 1024>(d1[pc & 3], sW1, so1 + rd * 64 * (FF * 2));
      }
      if constexpr (y >= 146 && y < 172) { ymicro(std::integral_constant<int, 2 * (y - 146)>{}); ymicro(std::integral_constant<int, 2 * (y - 146) + 1>{}); }
      if constexpr (y >= 178 && y < 204) { ymicro(std::integral_constant<int, 52 + 2 * (y - 178)>{}); ymicro(std::integral_constant<int, 53 + 2 * (y - 178)>{}); }
      if constexpr (y >= 210 && y < 223) static_for<4>([&](auto M_) INL { ymicro(std::integral_constant<int, 104 + 4 * (y - 210) + M_>{}); });
      GAP();
    });
    asm volatile("s_nop 7\n\ts_nop 7");
    static_for<26>([&](auto M_) INL { ymicro(std::integral_constant<int, 156 + M_>{}); });
    WAIT_VL(0, 15); GAP();
    PIN(xr[0]); PIN(xr[1]);
    signal_wg(flagp(ws, FL_Y + blk));
    GAP();
    dma_grp(d1, sW1, so1 + 6 * 64 * (FF * 2), ldsb + w * 4096 + 6 * 16384);
  }
  tail(y, xr, w1, w2, out, ws, ldsb, w);
  asm volatile("k1_code_end:");
}

extern "C" void kernel_launch(void* const* d_in, const int* in_sizes, int n_in, void* d_out, int out_size, void* d_ws, size_t ws_size,
                              hipStream_t stream) {
  (void)hipFuncSetAttribute((const void*)k1_block, hipFuncAttributeMaxDynamicSharedMemorySize, (int)LDS_BYTES);
  k1_block<<<256, 256, LDS_BYTES, stream>>>((const u16*)d_in[0], (const u16*)d_in[1], (const u16*)d_in[2], (const u16*)d_in[3], (const u16*)d_in[4],
                                            (const u16*)d_in[5], (const u16*)d_in[7], (const u16*)d_in[8], (u16*)d_out, (char*)d_ws + (((ws_size - WS_END) / 2) & ~(size_t)0x1fffff));
}
```
